```python
import torch

def _softmax(x, axis=-1):
    return torch.softmax(x, dim=axis)
def _gelu(x):
    return torch.nn.functional.gelu(x, approximate="tanh")
def _silu(x):
    return torch.nn.functional.silu(x)
def _sigmoid(x):
    return torch.sigmoid(x)
def _layernorm(x, axis=-1, eps=1e-5):
    m = x.mean(dim=axis, keepdim=True)
    v = x.var(dim=axis, keepdim=True, unbiased=False)
    return (x - m) / torch.sqrt(v + eps)
def _scatter_add(tgt, idx, src, axis=0):
    return tgt.clone().index_add_(axis, idx, src)
def _segment_sum(src, seg, n):
    out = torch.zeros((n,) + src.shape[1:], dtype=src.dtype)
    return out.index_add_(0, seg, src)
def _pad(x, pw):
    flat = tuple(v for pair in reversed(pw) for v in pair)
    return torch.nn.functional.pad(x, flat)

kernel_name = 'sequential_preLN__grouped_kv_mqa__moe_arch_mod__prompt'

def setup_inputs():

    _w = (lambda a: (lambda t: t.to(torch.bfloat16) if t.is_floating_point() else t)(torch.as_tensor(a)))
    def _A_setup():
        B, S, D, H = 1, 2048, 448, 16
        Dh = D // H
        return {
            "x": _w((lambda *s: torch.randn(*s, dtype=torch.bfloat16) * 0.05)(B, S, D)),
            "wq": _w((lambda *s: torch.randn(*s, dtype=torch.bfloat16) * 0.05)(D, H * Dh)),
            "wk": _w((lambda *s: torch.randn(*s, dtype=torch.bfloat16) * 0.05)(D, Dh)),
            "wv": _w((lambda *s: torch.randn(*s, dtype=torch.bfloat16) * 0.05)(D, Dh)),
            "wo": _w((lambda *s: torch.randn(*s, dtype=torch.bfloat16) * 0.05)(H * Dh, D)),
        }
    _A_in = _A_setup()

    def _B_setup():
        B, S, D, F, C = 1, 2048, 448, 1792, 2
        return {
            "x": _w((lambda *s: torch.randn(*s, dtype=torch.bfloat16) * 0.05)(B, S, D)),
            "w_r": _w((lambda *s: torch.randn(*s, dtype=torch.bfloat16) * 0.05)(D)),
            "w_gate": _w((lambda *s: torch.randn(*s, dtype=torch.bfloat16) * 0.05)(D, F)),
            "w_up": _w((lambda *s: torch.randn(*s, dtype=torch.bfloat16) * 0.05)(D, F)),
            "w_down": _w((lambda *s: torch.randn(*s, dtype=torch.bfloat16) * 0.05)(F, D)),
        }
    _B_in = _B_setup()

    return {
        "A_x": _A_in["x"],
        "A_wq": _A_in["wq"],
        "A_wk": _A_in["wk"],
        "A_wv": _A_in["wv"],
        "A_wo": _A_in["wo"],
        "B_x": _B_in["x"],
        "B_w_r": _B_in["w_r"],
        "B_w_gate": _B_in["w_gate"],
        "B_w_up": _B_in["w_up"],
        "B_w_down": _B_in["w_down"],
    }

def reference(A_x, A_wq, A_wk, A_wv, A_wo, B_x, B_w_r, B_w_gate, B_w_up, B_w_down):
    def _A_ref(x, wq, wk, wv, wo):
        B, S, D = x.shape
        H = 16
        Dh = D // H
        q = (lambda x, *d: x.permute(*d))((x @ wq).reshape(B, S, H, Dh), 0, 2, 1, 3)
        k = x @ wk
        v = x @ wv
        scores = torch.einsum("bhqd,bkd->bhqk", q, k) * (Dh ** -0.5)
        p = _softmax(scores, axis=-1)
        o = torch.einsum("bhqk,bkd->bhqd", p, v)
        return (lambda x, *d: x.permute(*d))(o, 0, 2, 1, 3).reshape(B, S, H * Dh) @ wo

    def _B_ref(x, w_r, w_gate, w_up, w_down):
        B, S, D = x.shape
        C = 2
        r = x @ w_r
        idx = torch.argsort(-r, axis=-1)[:, :C]
        idx_e = (lambda x, n, axis: torch.repeat_interleave(x, n, dim=axis))(idx.reshape(B, C, 1), D, axis=2)
        x_sel = (lambda x, i, axis: torch.take_along_dim(x, i, dim=axis))(x, idx_e, axis=1)
        g = _silu(x_sel @ w_gate)
        u = x_sel @ w_up
        y = (g * u) @ w_down
        r_sel = _sigmoid((lambda x, i, axis: torch.take_along_dim(x, i, dim=axis))(r, idx, axis=1))
        y = y * r_sel.reshape(B, C, 1)
        b_off = (torch.arange(B) * S).reshape(B, 1)
        flat_idx = (idx + b_off).reshape(B * C)
        zero = (x * 0.0).reshape(B * S, D)
        upd = _scatter_add(zero, flat_idx, y.reshape(B * C, D), axis=0)
        return x + upd.reshape(B, S, D)

    x = A_x
    h = x + _A_ref(_layernorm(x, axis=-1), A_wq, A_wk, A_wv, A_wo)
    h2 = h + _B_ref(_layernorm(h, axis=-1), B_w_r, B_w_gate, B_w_up, B_w_down)
    return h2
```

```cpp
#include <hip/hip_runtime.h>
#include <hip/hip_bf16.h>
#include <stdint.h>

constexpr int S = 2048, D = 448, H = 16, DH = 28, F = 1792;

__device__ __forceinline__ float bf2f(uint16_t b) { return __uint_as_float(((uint32_t)b) << 16); }
__device__ __forceinline__ uint16_t f2bf(float f) { __hip_bfloat16 b = __float2bfloat16(f); return *(uint16_t*)&b; }
__device__ __forceinline__ float rbf(float f) { return bf2f(f2bf(f)); }

__device__ float block_sum(float v, float* red) {
    for (int m = 32; m >= 1; m >>= 1) v += __shfl_xor(v, m);
    int w = threadIdx.x >> 6;
    __syncthreads();
    if ((threadIdx.x & 63) == 0) red[w] = v;
    __syncthreads();
    float s = 0;
    for (int i = 0; i < (int)(blockDim.x >> 6); ++i) s += red[i];
    return s;
}

__global__ void k_ln_qkv(const uint16_t* x, const uint16_t* wq, const uint16_t* wk, const uint16_t* wv, float* q, float* k, float* v) {
    __shared__ float xn[D];
    __shared__ float red[8];
    int t = blockIdx.x;
    float a = 0;
    for (int i = threadIdx.x; i < D; i += 256) a += bf2f(x[t * D + i]);
    float mean = block_sum(a, red) / D;
    float b = 0;
    for (int i = threadIdx.x; i < D; i += 256) { float d = bf2f(x[t * D + i]) - mean; b += d * d; }
    float var = block_sum(b, red) / D;
    float mb = rbf(mean), sd = rbf(sqrtf(rbf(rbf(var) + 1e-5f)));
    for (int i = threadIdx.x; i < D; i += 256) xn[i] = rbf(rbf(bf2f(x[t * D + i]) - mb) / sd);
    __syncthreads();
    for (int c = threadIdx.x; c < D + 2 * DH; c += 256) {
        float acc = 0;
        if (c < D) { for (int i = 0; i < D; ++i) acc += xn[i] * bf2f(wq[i * D + c]); q[t * D + c] = rbf(acc); }
        else if (c < D + DH) { int cc = c - D; for (int i = 0; i < D; ++i) acc += xn[i] * bf2f(wk[i * DH + cc]); k[t * DH + cc] = rbf(acc); }
        else { int cc = c - D - DH; for (int i = 0; i < D; ++i) acc += xn[i] * bf2f(wv[i * DH + cc]); v[t * DH + cc] = rbf(acc); }
    }
}

__global__ void k_attn(const float* q, const float* k, const float* v, float* o) {
    __shared__ float ks[64 * DH], vs[64 * DH];
    int t = blockIdx.x * 64 + threadIdx.x, h = blockIdx.y;
    float qr[DH], acc[DH];
    for (int d = 0; d < DH; ++d) { qr[d] = q[t * D + h * DH + d]; acc[d] = 0; }
    float m = -INFINITY, l = 0;
    const float sc = 0.18898223650461361f;
    for (int j0 = 0; j0 < S; j0 += 64) {
        __syncthreads();
        for (int i = threadIdx.x; i < 64 * DH; i += 64) { ks[i] = k[j0 * DH + i]; vs[i] = v[j0 * DH + i]; }
        __syncthreads();
        for (int j = 0; j < 64; ++j) {
            float s = 0;
            for (int d = 0; d < DH; ++d) s += qr[d] * ks[j * DH + d];
            s *= sc;
            float mn = fmaxf(m, s);
            float al = __expf(m - mn), p = __expf(s - mn);
            l = l * al + p;
            for (int d = 0; d < DH; ++d) acc[d] = acc[d] * al + p * vs[j * DH + d];
            m = mn;
        }
    }
    for (int d = 0; d < DH; ++d) o[t * D + h * DH + d] = rbf(acc[d] / l);
}

__global__ void k_oproj(const uint16_t* x, const float* o, const uint16_t* wo, const uint16_t* wr, float* hbuf, float* hn, float* r, uint16_t* out) {
    __shared__ float os[D], hs[D];
    __shared__ float red[8];
    int t = blockIdx.x;
    for (int i = threadIdx.x; i < D; i += 256) os[i] = o[t * D + i];
    __syncthreads();
    for (int c = threadIdx.x; c < D; c += 256) {
        float acc = 0;
        for (int i = 0; i < D; ++i) acc += os[i] * bf2f(wo[i * D + c]);
        hs[c] = rbf(bf2f(x[t * D + c]) + rbf(acc));
    }
    __syncthreads();
    float a = 0;
    for (int i = threadIdx.x; i < D; i += 256) a += hs[i];
    float mean = block_sum(a, red) / D;
    float b = 0;
    for (int i = threadIdx.x; i < D; i += 256) { float d = hs[i] - mean; b += d * d; }
    float var = block_sum(b, red) / D;
    float mb = rbf(mean), sd = rbf(sqrtf(rbf(rbf(var) + 1e-5f)));
    float rr = 0;
    for (int i = threadIdx.x; i < D; i += 256) {
        float n = rbf(rbf(hs[i] - mb) / sd);
        hn[t * D + i] = n; hbuf[t * D + i] = hs[i];
        rr += n * bf2f(wr[i]);
        out[t * D + i] = f2bf(hs[i] + n);
    }
    rr = block_sum(rr, red);
    if (threadIdx.x == 0) r[t] = rbf(rr);
}

__device__ void top2(const float* r, int* idx, float* val) {
    float b0 = -INFINITY, b1 = -INFINITY; int i0 = 0, i1 = 0;
    for (int i = 0; i < S; ++i) { float v = r[i]; if (v > b0) { b1 = b0; i1 = i0; b0 = v; i0 = i; } else if (v > b1) { b1 = v; i1 = i; } }
    idx[0] = i0; idx[1] = i1; val[0] = b0; val[1] = b1;
}

__global__ void k_ffn1(const float* r, const float* hn, const uint16_t* wg, const uint16_t* wu, float* act) {
    __shared__ int idx[2]; __shared__ float val[2];
    if (threadIdx.x == 0) top2(r, idx, val);
    __syncthreads();
    int c = threadIdx.x >> 6, f = blockIdx.x * 64 + (threadIdx.x & 63);
    const float* xr = hn + idx[c] * D;
    float g = 0, u = 0;
    for (int i = 0; i < D; ++i) { g += xr[i] * bf2f(wg[i * F + f]); u += xr[i] * bf2f(wu[i * F + f]); }
    g = rbf(g); u = rbf(u);
    float sg = rbf(g / (1.f + __expf(-g)));
    act[c * F + f] = rbf(sg * u);
}

__global__ void k_ffn2(const float* r, const float* hn, const float* hbuf, const float* act, const uint16_t* wd, uint16_t* out) {
    __shared__ int idx[2]; __shared__ float val[2];
    if (threadIdx.x == 0) top2(r, idx, val);
    __syncthreads();
    int c = threadIdx.x >> 6, d = blockIdx.x * 64 + (threadIdx.x & 63);
    float y = 0;
    for (int f = 0; f < F; ++f) y += act[c * F + f] * bf2f(wd[f * D + d]);
    y = rbf(y);
    float sg = rbf(1.f / (1.f + __expf(-val[c])));
    y = rbf(y * sg);
    int t = idx[c];
    out[t * D + d] = f2bf(hbuf[t * D + d] + rbf(hn[t * D + d] + y));
}

extern "C" void kernel_launch(void* const* d_in, const int* in_sizes, int n_in, void* d_out, int out_size, void* d_ws, size_t ws_size, hipStream_t stream) {
    const uint16_t* x = (const uint16_t*)d_in[0];
    const uint16_t* wq = (const uint16_t*)d_in[1];
    const uint16_t* wk = (const uint16_t*)d_in[2];
    const uint16_t* wv = (const uint16_t*)d_in[3];
    const uint16_t* wo = (const uint16_t*)d_in[4];
    const uint16_t* wr = (const uint16_t*)d_in[6];
    const uint16_t* wg = (const uint16_t*)d_in[7];
    const uint16_t* wu = (const uint16_t*)d_in[8];
    const uint16_t* wd = (const uint16_t*)d_in[9];
    uint16_t* out = (uint16_t*)d_out;
    float* ws = (float*)d_ws;
    float* q = ws; float* k = q + S * D; float* v = k + S * DH; float* o = v + S * DH; float* hb = o + S * D; float* hn = hb + S * D; float* r = hn + S * D; float* act = r + S;
    k_ln_qkv<<<S, 256, 0, stream>>>(x, wq, wk, wv, q, k, v);
    k_attn<<<dim3(S / 64, H), 64, 0, stream>>>(q, k, v, o);
    k_oproj<<<S, 256, 0, stream>>>(x, o, wo, wr, hb, hn, r, out);
    k_ffn1<<<F / 64, 128, 0, stream>>>(r, hn, wg, wu, act);
    k_ffn2<<<D / 64, 128, 0, stream>>>(r, hn, hb, act, wd, out);
}
```

```cpp
#include <hip/hip_runtime.h>
#include <stdint.h>

#define GAS __attribute__((address_space(1)))
#define LAS __attribute__((address_space(3)))
#define CAS __attribute__((address_space(4)))
typedef unsigned v4u __attribute__((ext_vector_type(4)));
typedef unsigned v2u __attribute__((ext_vector_type(2)));
typedef float f32x16 __attribute__((ext_vector_type(16)));
typedef float f32x8 __attribute__((ext_vector_type(8)));
typedef float f32x4 __attribute__((ext_vector_type(4)));
typedef float f32x2 __attribute__((ext_vector_type(2)));
typedef __bf16 bf16x2 __attribute__((ext_vector_type(2)));
typedef __bf16 bf16x8 __attribute__((ext_vector_type(8)));
typedef __bf16 bf16x16 __attribute__((ext_vector_type(16)));
typedef short v4i16 __attribute__((ext_vector_type(4)));
typedef GAS unsigned gu32;
typedef GAS unsigned long long gu64;
#define RLX_AGENT __ATOMIC_RELAXED, __HIP_MEMORY_SCOPE_AGENT

namespace {
constexpr int S = 2048, D = 448, DH = 28, F = 1792;
constexpr int NWG = 256, NTHR = 512;
constexpr float QSCALE = 0.18898223650461361f * 1.4426950408889634f;
constexpr float EPS = 1e-5f;
constexpr unsigned SPIN_CAP = 1u << 18;

constexpr unsigned WS_EPOCH = 0;
constexpr unsigned WS_FLAG = 4096;
constexpr unsigned WS_CAND = WS_FLAG + 16384;
constexpr unsigned WS_ACT = WS_CAND + 8192;
constexpr unsigned WS_KV = 65536;
constexpr unsigned WS_STAT = WS_KV + 262144;
constexpr unsigned WS_ROW = WS_STAT + 262144;
constexpr unsigned WS_O = WS_ROW + 128 * 3584;
constexpr unsigned WS_END = WS_O + 256 * 14336;
constexpr unsigned WS_DBG = 8u << 20;

constexpr unsigned L_XI = 0;
constexpr unsigned L_RING = 28672;
constexpr unsigned L_WKV = L_RING + 4 * 24576;
constexpr unsigned L_QI = L_WKV + 25600;
constexpr unsigned L_MISC = L_QI + 8192;
constexpr unsigned L_STAT = L_MISC;
constexpr unsigned L_CSKV = L_STAT + 256;
constexpr unsigned L_CSQ = L_CSKV + 128;
constexpr unsigned L_PST = L_CSQ + 512;
constexpr unsigned L_WIN = L_PST + 2048;
constexpr unsigned LDS_BYTES = L_WIN + 128;
constexpr unsigned L_WST = L_RING;
constexpr unsigned L_RED = L_WST + 4 * 14336;
constexpr unsigned L_FS = L_RED + 49152;
static_assert(LDS_BYTES <= 163840 && L_FS + 8192 <= L_QI, "lds");

struct Params {
    const uint16_t* x; const uint16_t* wq; const uint16_t* wk; const uint16_t* wv; const uint16_t* wo;
    const uint16_t* wr; const uint16_t* wg; const uint16_t* wu; const uint16_t* wd;
    uint16_t* out; unsigned char* ws;
};

#define SBAR() __builtin_amdgcn_sched_barrier(0)
#define BAR() asm volatile("s_waitcnt lgkmcnt(0)\n\ts_barrier" ::: "memory")
#define VMWAIT(n) asm volatile("s_waitcnt vmcnt(" #n ")" ::: "memory")
#define LGWAIT() asm volatile("s_waitcnt lgkmcnt(0)" ::: "memory")

__device__ __forceinline__ v4u mk_srd(const void* p, unsigned bytes) {
    const unsigned long long a = (unsigned long long)p;
    v4u r; r.x = (unsigned)a; r.y = (unsigned)(a >> 32) & 0xffffu; r.z = bytes; r.w = 0x00020000u; return r;
}
__device__ __forceinline__ void dma16(v4u srd, unsigned voff, unsigned soff, unsigned ldsaddr) {
    asm volatile("s_mov_b32 m0, %0\n\ts_nop 0\n\tbuffer_load_dwordx4 %1, %2, %3 offen lds" :: "s"(ldsaddr), "v"(voff), "s"(srd), "s"(soff) : "memory");
}
__device__ __forceinline__ unsigned pk(float lo, float hi) {
    f32x2 v = {lo, hi}; return __builtin_bit_cast(unsigned, __builtin_convertvector(v, bf16x2));
}
__device__ __forceinline__ float bflo(unsigned u) { return __uint_as_float(u << 16); }
__device__ __forceinline__ float bfhi(unsigned u) { return __uint_as_float(u & 0xffff0000u); }
__device__ __forceinline__ float rbf(float f) { return (float)(__bf16)f; }
__device__ __forceinline__ v2u trd(LAS char* p) { return __builtin_bit_cast(v2u, __builtin_amdgcn_ds_read_tr16_b64_v4i16((LAS v4i16*)p)); }
__device__ __forceinline__ bf16x8 mk8(v2u a, v2u b) { v4u r = {a.x, a.y, b.x, b.y}; return __builtin_bit_cast(bf16x8, r); }
__device__ __forceinline__ bf16x8 as8(v4u a) { return __builtin_bit_cast(bf16x8, a); }
__device__ __forceinline__ float dot2(unsigned a, unsigned b, float c) { return __builtin_amdgcn_fdot2_f32_bf16(__builtin_bit_cast(bf16x2, a), __builtin_bit_cast(bf16x2, b), c, false); }
template <int CTRL> __device__ __forceinline__ float dppf(float x) { return __builtin_bit_cast(float, __builtin_amdgcn_mov_dpp(__builtin_bit_cast(int, x), CTRL, 0xf, 0xf, true)); }
template <int CTRL> __device__ __forceinline__ unsigned dppu(unsigned x) { return (unsigned)__builtin_amdgcn_mov_dpp((int)x, CTRL, 0xf, 0xf, true); }
constexpr int XOR1 = 0xB1, XOR2 = 0x4E, XOR7 = 0x141, ROR8 = 0x128, MIR15 = 0x140;
__device__ __forceinline__ float half_sum(float v) { auto r = __builtin_amdgcn_permlane32_swap(__float_as_uint(v), __float_as_uint(v), false, false); return __uint_as_float(r[0]) + __uint_as_float(r[1]); }
__device__ __forceinline__ unsigned umax32(unsigned v) {
    v = max(v, dppu<XOR1>(v)); v = max(v, dppu<XOR2>(v)); v = max(v, dppu<XOR7>(v)); v = max(v, dppu<MIR15>(v));
    auto r = __builtin_amdgcn_permlane16_swap(v, v, false, false); return max(r[0], r[1]);
}
__device__ __forceinline__ unsigned umax64(unsigned v) { v = umax32(v); auto r = __builtin_amdgcn_permlane32_swap(v, v, false, false); return max(r[0], r[1]); }
#define MFMA32(a, b, c) __builtin_amdgcn_mfma_f32_32x32x16_bf16(a, b, c, 0, 0, 0)
#define MFMA16(a, b, c) __builtin_amdgcn_mfma_f32_16x16x32_bf16(a, b, c, 0, 0, 0)
__device__ __forceinline__ f32x16 smf_lo(bf16x8 v, const f32x16& p, f32x16 o, int idx) {
    f32x8 h = __builtin_shufflevector(p, p, 0, 1, 2, 3, 4, 5, 6, 7);
    return __builtin_amdgcn_smfmac_f32_32x32x32_bf16(v, __builtin_bit_cast(bf16x16, h), o, idx, 0, 0);
}
__device__ __forceinline__ f32x16 smf_hi(bf16x8 v, const f32x16& p, f32x16 o, int idx) {
    f32x8 h = __builtin_shufflevector(p, p, 8, 9, 10, 11, 12, 13, 14, 15);
    return __builtin_amdgcn_smfmac_f32_32x32x32_bf16(v, __builtin_bit_cast(bf16x16, h), o, idx, 0, 0);
}
#define LDSP(T, off) ((LAS T*)(L + (off)))
#define BLOAD(off, soff) __builtin_amdgcn_raw_buffer_load_b128(wsr, (off), (soff), 16)
#define BSTORE(v, off, soff) __builtin_amdgcn_raw_buffer_store_b128((v), wsr, (off), (soff), 16)

#define STAMP(i) do {} while (0)
#define STAMPC(i) do {} while (0)
#define STAMP_DECL do {} while (0)
#define STAMP_FLUSH() do {} while (0)
#define POLLMEM() asm volatile("" ::: "memory")

__global__ __launch_bounds__(NTHR, 2) void fused_kernel(Params p) {
    __shared__ __attribute__((aligned(16))) char lds_[LDS_BYTES];
    LAS char* L = (LAS char*)lds_;
    const unsigned lbase = (unsigned)(uintptr_t)L;
    const int g = blockIdx.x, xcd = g & 7, jj = g >> 3, cl = xcd * 8 + (jj >> 2), m = jj & 3, t0 = cl * 32;
    const int tid = threadIdx.x, lane = tid & 63, wave = __builtin_amdgcn_readfirstlane(tid >> 6);
    const int l31 = lane & 31, hh = lane >> 5, l15 = lane & 15, gq = lane >> 4;
    const int w3 = wave & 3;
    STAMP_DECL;
    STAMP(0);
    const unsigned tag = *(const CAS unsigned*)(p.ws + WS_EPOCH) + 1u;
    const __amdgpu_buffer_rsrc_t wsr = __builtin_amdgcn_make_buffer_rsrc(p.ws, 0, WS_END, 0x00020000);

    {
        const v4u sx = mk_srd(p.x + (size_t)t0 * D, 32 * D * 2);
        const v4u sw = mk_srd((m & 1) ? p.wv : p.wk, D * DH * 2);
        const int row = 8 * w3 + (lane >> 3);
        const unsigned vx = row * (D * 2) + (((lane & 7) ^ ((row >> 1) & 7)) << 4);
        if (wave < 4) {
#pragma unroll
            for (int cb = 0; cb < 4; ++cb) dma16(sx, vx, cb * 128, lbase + L_XI + (w3 * 7 + cb) * 1024);
#pragma unroll
            for (int i = 0; i < 3; ++i) dma16(sw, lane * 16, (3 * w3 + i) * 1024, lbase + L_WKV + (3 * w3 + i) * 1024);
        } else {
#pragma unroll
            for (int cb = 4; cb < 7; ++cb) dma16(sx, vx, cb * 128, lbase + L_XI + (w3 * 7 + cb) * 1024);
#pragma unroll
            for (int i = 0; i < 3; ++i) dma16(sw, lane * 16, (12 + w3 + 4 * i) * 1024, lbase + L_WKV + (12 + w3 + 4 * i) * 1024);
            if (wave == 4) dma16(sw, lane * 16, 24 * 1024, lbase + L_WKV + 24 * 1024);
        }
    }
    const unsigned cbase = (w3 * 7) >> 1;
    const unsigned vw = (lane >> 2) * (D * 2) + 224 * m + ((cbase + (lane & 3)) << 4);
    const unsigned trofs = (8 * hh + ((lane & 15) >> 2)) * 64 + 8 * (w3 & 1) + 32 * ((lane >> 4) & 1) + 8 * (lane & 3);

    if (wave < 4) {
        const v4u sq = mk_srd(p.wq, D * D * 2);
        const unsigned ring = L_RING + w3 * 24576;
#pragma unroll
        for (int s = 0; s < 24; ++s) dma16(sq, vw, s * 16 * D * 2, lbase + ring + s * 1024);
        VMWAIT(24);
        BAR();
        STAMP(1);
        {
            float s1 = 0.f, s2 = 0.f;
#pragma unroll
            for (int cb = 0; cb < 7; ++cb) {
                const v4u v = *LDSP(v4u, L_XI + (w3 * 7 + cb) * 1024 + lane * 16);
#pragma unroll
                for (int e = 0; e < 4; ++e) { s1 = dot2(v[e], 0x3f803f80u, s1); s2 = dot2(v[e], v[e], s2); }
            }
            s1 += dppf<XOR1>(s1); s2 += dppf<XOR1>(s2); s1 += dppf<XOR2>(s1); s2 += dppf<XOR2>(s2); s1 += dppf<XOR7>(s1); s2 += dppf<XOR7>(s2);
            const float mean = s1 * (1.f / D), var = fmaxf(s2 * (1.f / D) - mean * mean, 0.f);
            const float sd = rbf(sqrtf(rbf(rbf(var) + EPS)));
            if ((lane & 7) == 0) *LDSP(f32x2, L_STAT + (8 * w3 + (lane >> 3)) * 8) = (f32x2){rbf(mean), 1.f / sd};
        }
        BAR();
        STAMP(2);
        f32x16 acc = {};
        float cs = 0.f;
        unsigned xa[4];
#pragma unroll
        for (int i = 0; i < 4; ++i) xa[i] = L_XI + (l31 >> 3) * 7168 + (l31 & 7) * 128 + (((2 * i + hh) ^ ((l31 >> 1) & 7)) << 4);
#define QSTEP(s, slot) do { \
            const v2u a0 = trd(L + ring + (slot) * 1024 + trofs), a1 = trd(L + ring + (slot) * 1024 + trofs + 256); \
            const v4u xb = *LDSP(v4u, xa[(s) & 3] + ((s) >> 2) * 1024); \
            acc = MFMA32(mk8(a0, a1), as8(xb), acc); \
            cs = dot2(a0.x, 0x3f803f80u, cs); cs = dot2(a0.y, 0x3f803f80u, cs); cs = dot2(a1.x, 0x3f803f80u, cs); cs = dot2(a1.y, 0x3f803f80u, cs); } while (0)
        VMWAIT(20);
        QSTEP(0, 0); QSTEP(1, 1); QSTEP(2, 2); QSTEP(3, 3);
        LGWAIT();
#pragma unroll
        for (int s = 24; s < 28; ++s) dma16(sq, vw, s * 16 * D * 2, lbase + ring + (s - 24) * 1024);
        VMWAIT(20); QSTEP(4, 4); QSTEP(5, 5); QSTEP(6, 6); QSTEP(7, 7);
        VMWAIT(16); QSTEP(8, 8); QSTEP(9, 9); QSTEP(10, 10); QSTEP(11, 11);
        VMWAIT(12); QSTEP(12, 12); QSTEP(13, 13); QSTEP(14, 14); QSTEP(15, 15);
        VMWAIT(8); QSTEP(16, 16); QSTEP(17, 17); QSTEP(18, 18); QSTEP(19, 19);
        VMWAIT(4); QSTEP(20, 20); QSTEP(21, 21); QSTEP(22, 22); QSTEP(23, 23);
        VMWAIT(0); QSTEP(24, 0); QSTEP(25, 1); QSTEP(26, 2); QSTEP(27, 3);
#undef QSTEP
        cs = half_sum(cs);
        if (hh == 0) *LDSP(float, L_CSQ + w3 * 128 + l31 * 4) = cs;
        LGWAIT();
        {
            const f32x2 st = *LDSP(f32x2, L_STAT + l31 * 8);
            const float sc = st.y * QSCALE;
            unsigned qp[8];
#pragma unroll
            for (int i = 0; i < 4; ++i) {
                const f32x4 c4 = *LDSP(f32x4, L_CSQ + w3 * 128 + (8 * i + 4 * hh) * 4);
                float q[4];
#pragma unroll
                for (int r = 0; r < 4; ++r) q[r] = (acc[4 * i + r] - st.x * c4[r]) * sc;
                qp[2 * i] = pk(q[0], q[1]); qp[2 * i + 1] = pk(q[2], q[3]);
            }
            if (hh) { qp[6] = 0u; qp[7] = 0u; }
            *LDSP(v4u, L_QI + (2 * w3 + 0) * 1024 + lane * 16) = (v4u){qp[0], qp[1], qp[4], qp[5]};
            *LDSP(v4u, L_QI + (2 * w3 + 1) * 1024 + lane * 16) = (v4u){qp[2], qp[3], qp[6], qp[7]};
        }
        BAR();
        STAMP(3);
        bf16x8 Q[4][2];
#pragma unroll
        for (int i = 0; i < 4; ++i) { Q[i][0] = as8(*LDSP(v4u, L_QI + (2 * i) * 1024 + lane * 16)); Q[i][1] = as8(*LDSP(v4u, L_QI + (2 * i + 1) * 1024 + lane * 16)); }
        const int rot = (g >> 2) & 15;
        {
            const unsigned fo = WS_FLAG + ((((4 * ((lane >> 2) + rot) + w3) & 63) * 4 + (lane & 3)) << 6);
            unsigned spins = 0;
            while (!__all(__hip_atomic_load((gu32*)(p.ws + fo), RLX_AGENT) == tag) && ++spins < SPIN_CAP) {}
            __builtin_amdgcn_fence(__ATOMIC_ACQUIRE, "wavefront");
        }
        STAMP(4);
        f32x16 O[4] = {}, Sb[4];
        v4u KB[4][2], VB[4][2];
        const unsigned kvo = WS_KV + lane * 16;
        const int idx = (int)0xDDDDDDDDu;
#define TILE_OFF(i) ((i) < 16 ? (unsigned)(((4 * ((i) + rot) + w3) & 63) << 12) : 0x7ff00000u)
#define LDK(b, i) do { const unsigned so_ = TILE_OFF(i); KB[b][0] = BLOAD(kvo, so_); KB[b][1] = BLOAD(kvo + 1024, so_); } while (0)
#define LDV(b, i) do { const unsigned so_ = TILE_OFF(i); VB[b][0] = BLOAD(kvo + 2048, so_); VB[b][1] = BLOAD(kvo + 3072, so_); } while (0)
        LDK(0, 0); LDV(0, 0); LDK(1, 1); LDV(1, 1); LDK(2, 2); LDV(2, 2);
        VB[3][1] = (v4u){0u, 0u, 0u, 0u};
        Sb[3] = (f32x16){};
        Sb[0] = MFMA32(as8(KB[0][0]), Q[0][0], (f32x16){});
        Sb[0] = MFMA32(as8(KB[0][1]), Q[0][1], Sb[0]);
#define EXP4(X, o) do { X[o] = __builtin_amdgcn_exp2f(X[o]); X[o + 1] = __builtin_amdgcn_exp2f(X[o + 1]); X[o + 2] = __builtin_amdgcn_exp2f(X[o + 2]); X[o + 3] = __builtin_amdgcn_exp2f(X[o + 3]); } while (0)
#define UNIT(b, q, nb, pb) do { \
            Sb[((q) + 1) & 3] = MFMA32(as8(KB[nb][0]), Q[((q) + 1) & 3][0], (f32x16){}); SBAR(); EXP4(Sb[q], 0); SBAR(); \
            Sb[((q) + 1) & 3] = MFMA32(as8(KB[nb][1]), Q[((q) + 1) & 3][1], Sb[((q) + 1) & 3]); SBAR(); EXP4(Sb[q], 4); SBAR(); \
            O[((q) + 3) & 3] = smf_hi(as8(VB[pb][1]), Sb[((q) + 3) & 3], O[((q) + 3) & 3], idx); SBAR(); EXP4(Sb[q], 8); SBAR(); \
            O[q] = smf_lo(as8(VB[b][0]), Sb[q], O[q], idx); SBAR(); EXP4(Sb[q], 12); SBAR(); } while (0)
#define TILE(b, i) do { \
            LDK(((b) + 3) & 3, (i) + 3); SBAR(); \
            UNIT(b, 0, b, ((b) + 3) & 3); \
            LDV(((b) + 3) & 3, (i) + 3); SBAR(); \
            UNIT(b, 1, b, b); UNIT(b, 2, b, b); UNIT(b, 3, ((b) + 1) & 3, b); } while (0)
        STAMPC(14);
#pragma unroll 1
        for (int it = 0; it < 16; it += 4) { TILE(0, it); TILE(1, it + 1); TILE(2, it + 2); TILE(3, it + 3); }
        O[3] = smf_hi(as8(VB[3][1]), Sb[3], O[3], idx);
        STAMPC(15);
#undef UNIT
#undef TILE
#undef EXP4
#undef LDK
#undef LDV
        STAMP(5);
#pragma unroll
        for (int q = 0; q < 4; ++q) {
            if (q != w3) {
                const unsigned slot = q * 3 + ((w3 - q - 1) & 3);
#pragma unroll
                for (int i = 0; i < 4; ++i) *LDSP(f32x4, L_RED + slot * 4096 + i * 1024 + lane * 16) = (f32x4){O[q][4 * i], O[q][4 * i + 1], O[q][4 * i + 2], O[q][4 * i + 3]};
            }
        }
        f32x16 o = w3 == 0 ? O[0] : w3 == 1 ? O[1] : w3 == 2 ? O[2] : O[3];
        BAR();
#pragma unroll
        for (int s = 0; s < 3; ++s)
#pragma unroll
            for (int i = 0; i < 4; ++i) {
                const f32x4 v = *LDSP(f32x4, L_RED + (w3 * 3 + s) * 4096 + i * 1024 + lane * 16);
                o[4 * i] += v[0]; o[4 * i + 1] += v[1]; o[4 * i + 2] += v[2]; o[4 * i + 3] += v[3];
            }
        {
            auto r = __builtin_amdgcn_permlane32_swap(__float_as_uint(o[12]), __float_as_uint(o[12]), false, false);
            const float lsum = __uint_as_float(r[1]);
            const float rl = 1.f / lsum;
#pragma unroll
            for (int i = 0; i < 4; ++i) {
                const unsigned lo = pk(o[4 * i] * rl, o[4 * i + 1] * rl), hi = pk(o[4 * i + 2] * rl, o[4 * i + 3] * rl);
                const int qd = 2 * i + hh;
                if (i < 3 || hh == 0) {
                    BSTORE(((v4u){lo, tag, hi, tag}), WS_O + (7 * w3 + qd) * 512 + l31 * 16, (cl * 4 + m) * 14336);
                    const int k4 = 28 * m + 7 * w3 + qd;
                    *LDSP(v2u, L_XI + (k4 >> 2) * 1024 + (l31 + 32 * ((k4 >> 1) & 1)) * 16 + 8 * (k4 & 1)) = (v2u){lo, hi};
                }
            }
        }
        STAMP(6);
        BAR();
        BAR();
        STAMP(7);
        if (wave == 1) {
            f32x4 a = *LDSP(f32x4, L_PST + l31 * 16);
#pragma unroll
            for (int i = 1; i < 4; ++i) a += *LDSP(f32x4, L_PST + i * 512 + l31 * 16);
            const v4u v = hh ? (v4u){__float_as_uint(a[2]), tag, 0u, tag} : (v4u){__float_as_uint(a[0]), tag, __float_as_uint(a[1]), tag};
            BSTORE(v, WS_STAT + lane * 16, (cl * 4 + m) * 1024);
        }
        const bool isA = jj < 28;
        const int fb = xcd * 4 + (jj - 28);
        const bool isB = !isA && fb < 28;
        unsigned w0 = 0, w1 = 0;
        int c0 = 0, c1 = 0;
        bf16x8 WF[14];
        if ((isA && wave == 0) || isB) {
            const unsigned rw = 8 * gq + ((lane & 15) >> 2);
            const unsigned wbase = L_WST + w3 * 14336 + (isA ? ((lane >> 1) & 1) * 7168 + rw * 16 + 8 * (lane & 1) : rw * 32 + 8 * (lane & 3));
            const unsigned wstep = isA ? 512 : 1024;
#pragma unroll
            for (int s = 0; s < 14; ++s) WF[s] = mk8(trd(L + wbase + s * wstep), trd(L + wbase + s * wstep + (wstep >> 3)));
        }
        if (wave == 0) {
            v4u cv; unsigned spins = 0;
            do { POLLMEM(); cv = BLOAD(WS_CAND + lane * 128, 0); } while (!__all(cv.y == tag && cv.w == tag) && ++spins < SPIN_CAP);
            STAMP(8);
            w0 = umax64(cv.x);
            w1 = umax64(cv.x == w0 ? cv.z : cv.x);
            const unsigned long long b0 = __ballot(cv.x == w0), b1a = __ballot(cv.x == w1), b1b = __ballot(cv.z == w1);
            c0 = 2 * (int)__builtin_ctzll(b0);
            c1 = b1a ? 2 * (int)__builtin_ctzll(b1a) : 2 * (int)__builtin_ctzll(b1b | (1ull << 63)) + 1;
            if (lane == 0) *LDSP(v2u, L_WIN) = (v2u){w0, w1};
            if (isA) {
                v4u r[4]; spins = 0; bool ok;
                do {
                    ok = true; POLLMEM();
#pragma unroll
                    for (int i = 0; i < 4; ++i) {
                        const int u = lane + 64 * (i & 1);
                        r[i] = BLOAD(WS_ROW + (u < 112 ? u : 0) * 16, (i < 2 ? c0 : c1) * 3584);
                    }
#pragma unroll
                    for (int i = 0; i < 4; ++i) ok &= (r[i].y == tag && r[i].w == tag);
                } while (!__all(ok) && ++spins < SPIN_CAP);
                STAMP(9);
#pragma unroll
                for (int i = 0; i < 4; ++i) {
                    const int u = lane + 64 * (i & 1);
                    if (u < 112) *LDSP(v2u, L_FS + (i >> 1) * 896 + u * 8) = (v2u){r[i].x, r[i].z};
                }
                LGWAIT();
                f32x4 y = {};
#pragma unroll
                for (int s = 0; s < 14; ++s) y = MFMA16(as8(*LDSP(v4u, L_FS + (lane & 1) * 896 + s * 64 + gq * 16)), WF[s], y);
                const float u0 = dppf<ROR8>(y[0]), u1 = dppf<ROR8>(y[1]);
                const float g0 = rbf(y[0]), g1 = rbf(y[1]);
                const float a0 = rbf(rbf(g0 / (1.f + __expf(-g0))) * rbf(u0)), a1 = rbf(rbf(g1 / (1.f + __expf(-g1))) * rbf(u1));
                const float n0 = dppf<XOR1>(a0), n1 = dppf<XOR1>(a1);
                const unsigned pay = (lane & 1) ? pk(n1, a1) : pk(a0, n0);
                if (lane < 8) __hip_atomic_store((gu64*)(p.ws + WS_ACT + (xcd * 28 + jj) * 64 + (lane & 1) * 32 + (lane >> 1) * 8), ((unsigned long long)tag << 32) | pay, RLX_AGENT);
                STAMP(10);
            }
        }
        float hres = 0.f, hnres = 0.f;
        if (isB) {
            if (wave == 0) {
                if (fb == 0 && lane == 0) __hip_atomic_store((gu32*)(p.ws + WS_EPOCH), tag, RLX_AGENT);
                unsigned long long gn, gh; unsigned spins = 0;
                const unsigned ro = WS_ROW + ((lane & 16) ? c1 : c0) * 3584 + (fb * 8 + (l15 >> 1)) * 8;
                do { gn = __hip_atomic_load((gu64*)(p.ws + ro), RLX_AGENT); gh = __hip_atomic_load((gu64*)(p.ws + ro + 1792), RLX_AGENT); }
                while (!__all((unsigned)(gn >> 32) == tag && (unsigned)(gh >> 32) == tag) && ++spins < SPIN_CAP);
                hnres = (lane & 1) ? bfhi((unsigned)gn) : bflo((unsigned)gn);
                hres = (lane & 1) ? bfhi((unsigned)gh) : bflo((unsigned)gh);
            }
            v4u r[4]; unsigned spins = 0; bool ok;
            do {
                ok = true; POLLMEM();
#pragma unroll
                for (int i = 0; i < 4; ++i) { const int u = lane + 64 * i; r[i] = BLOAD(WS_ACT + (u < 224 ? u : 0) * 16, w3 * 3584); }
#pragma unroll
                for (int i = 0; i < 4; ++i) ok &= (r[i].y == tag && r[i].w == tag);
            } while (!__all(ok) && ++spins < SPIN_CAP);
            STAMP(11);
#pragma unroll
            for (int i = 0; i < 4; ++i) { const int u = lane + 64 * i; if (u < 224) *LDSP(v2u, L_FS + w3 * 1792 + u * 8) = (v2u){r[i].x, r[i].z}; }
            LGWAIT();
            f32x4 y = {};
#pragma unroll
            for (int s = 0; s < 14; ++s) y = MFMA16(as8(*LDSP(v4u, L_FS + w3 * 1792 + (4 * s + gq) * 32 + (lane & 1) * 16)), WF[s], y);
            if (lane < 16) *LDSP(f32x2, L_FS + 7168 + w3 * 128 + lane * 8) = (f32x2){y[0], y[1]};
        }
        BAR();
        if (isB && wave == 0 && lane < 32) {
            float y = 0.f;
#pragma unroll
            for (int i = 0; i < 4; ++i) y += *LDSP(float, L_FS + 7168 + i * 128 + l15 * 8 + gq * 4);
            const unsigned wk = gq ? w1 : w0;
            const unsigned rb = (wk >> 16) ^ ((wk & 0x80000000u) ? 0x8000u : 0xffffu);
            const float r = bflo(rb);
            const float sg = rbf(1.f / (1.f + __expf(-r)));
            const float res = hres + rbf(hnres + rbf(rbf(y) * sg));
            const int tok = 2047 - (int)(wk & 2047u);
            p.out[(size_t)tok * D + fb * 16 + l15] = __builtin_bit_cast(uint16_t, (__bf16)res);
        }
        STAMP(12);
        STAMP_FLUSH();
    } else {
        VMWAIT(0);
        BAR();
        STAMP(1);
        f32x4 ka[2] = {};
        if (wave < 6) {
            const unsigned wa = L_WKV + (8 * gq + ((lane & 15) >> 2)) * 56 + 8 * (lane & 3);
            const int tk = 16 * (m >> 1) + l15;
            unsigned xa[2];
#pragma unroll
            for (int i = 0; i < 2; ++i) xa[i] = L_XI + (tk >> 3) * 7168 + (tk & 7) * 128 + (((4 * i + gq) ^ ((tk >> 1) & 7)) << 4);
            const v4u ones = {0x3f803f80u, 0x3f803f80u, 0x3f803f80u, 0x3f803f80u};
#define KVW0(s) mk8(trd(L + wa + (s) * 1792), trd(L + wa + (s) * 1792 + 224))
#define KVW1(s) mk8(trd(L + wa + (s) * 1792 + 32), trd(L + wa + (s) * 1792 + 224 + 32))
#define KVX(s) as8(*LDSP(v4u, xa[(s) & 1] + ((s) >> 1) * 1024))
            if (wave == 5) {
#pragma unroll
                for (int s = 0; s < 14; ++s) { ka[0] = MFMA16(as8(ones), KVW0(s), ka[0]); ka[1] = MFMA16(as8(ones), KVW1(s), ka[1]); }
            } else if (m & 1) {
#pragma unroll
                for (int s = 0; s < 14; ++s) { const bf16x8 xb = KVX(s); ka[0] = MFMA16(xb, KVW0(s), ka[0]); ka[1] = MFMA16(xb, KVW1(s), ka[1]); }
            } else {
#pragma unroll
                for (int s = 0; s < 14; ++s) { const bf16x8 xb = KVX(s); ka[0] = MFMA16(KVW0(s), xb, ka[0]); ka[1] = MFMA16(KVW1(s), xb, ka[1]); }
            }
            if (wave == 5 && lane < 16) { *LDSP(float, L_CSKV + lane * 4) = ka[0][0]; *LDSP(float, L_CSKV + 64 + lane * 4) = ka[1][0]; }
        }
        BAR();
        STAMP(2);
        if (wave == 4) {
            v4u piece; unsigned dst;
            if (!(m & 1)) {
                const f32x2 st = *LDSP(f32x2, L_STAT + (16 * (m >> 1) + l15) * 8);
                const f32x4 c0 = *LDSP(f32x4, L_CSKV + gq * 16), c1 = *LDSP(f32x4, L_CSKV + 64 + gq * 16);
                float k[8];
#pragma unroll
                for (int r = 0; r < 4; ++r) { k[r] = (ka[0][r] - st.x * c0[r]) * st.y; k[4 + r] = (ka[1][r] - st.x * c1[r]) * st.y; }
                piece = (v4u){pk(k[0], k[1]), pk(k[2], k[3]), pk(k[4], k[5]), pk(k[6], k[7])};
                if (gq == 3) { piece.z = 0x3f803f80u; piece.w = 0x00003f80u; }
                dst = (gq >> 1) * 1024 + (16 * (m >> 1) + l15 + 32 * (gq & 1)) * 16;
            } else {
                const f32x4 sa = *LDSP(f32x4, L_STAT + (16 * (m >> 1) + 4 * gq) * 8), sb = *LDSP(f32x4, L_STAT + (16 * (m >> 1) + 4 * gq) * 8 + 16);
                const float mean[4] = {sa[0], sa[2], sb[0], sb[2]}, rs[4] = {sa[1], sa[3], sb[1], sb[3]};
                const float c0 = *LDSP(float, L_CSKV + l15 * 4), c1 = *LDSP(float, L_CSKV + 64 + l15 * 4);
                float v0[4], v1[4];
#pragma unroll
                for (int r = 0; r < 4; ++r) { v0[r] = (ka[0][r] - mean[r] * c0) * rs[r]; v1[r] = (ka[1][r] - mean[r] * c1) * rs[r]; }
                unsigned ax = pk(v0[0], v0[1]), ay = pk(v0[2], v0[3]), bx = pk(v1[0], v1[1]), by = pk(v1[2], v1[3]);
                if (l15 >= 12) { bx = by = (l15 == 12) ? 0x3f803f80u : 0u; }
                auto rx = __builtin_amdgcn_permlane16_swap(ax, bx, false, false);
                auto ry = __builtin_amdgcn_permlane16_swap(ay, by, false, false);
                piece = (v4u){rx[0], ry[0], rx[1], ry[1]};
                dst = 2048 + (m >> 1) * 1024 + lane * 16;
            }
            BSTORE(piece, WS_KV + dst, cl * 4096);
            VMWAIT(0);
            if (lane == 0) __hip_atomic_store((gu32*)(p.ws + WS_FLAG + (cl * 4 + m) * 64), tag, RLX_AGENT);
            STAMP(13);
        }
        v2u xr[4], wrq[4];
#pragma unroll
        for (int i = 0; i < 4; ++i) {
            const int c4 = 28 * m + 7 * w3 + min(2 * i + hh, 6);
            xr[i] = *LDSP(v2u, L_XI + ((l31 >> 3) * 7 + (c4 >> 4)) * 1024 + (l31 & 7) * 128 + ((((c4 >> 1) & 7) ^ ((l31 >> 1) & 7)) << 4) + 8 * (c4 & 1));
            wrq[i] = *(const GAS v2u*)(p.wr + c4 * 4);
        }
        float wsum;
        {
            float s = 0.f;
            if (lane < 56) { const v4u v = *(const GAS v4u*)(p.wr + lane * 8);
#pragma unroll
                for (int e = 0; e < 4; ++e) s = dot2(v[e], 0x3f803f80u, s); }
            s += dppf<XOR1>(s); s += dppf<XOR2>(s); s += dppf<XOR7>(s); s += dppf<MIR15>(s);
            auto r = __builtin_amdgcn_permlane16_swap(__float_as_uint(s), __float_as_uint(s), false, false); s = __uint_as_float(r[0]) + __uint_as_float(r[1]);
            wsum = half_sum(s);
        }
        BAR();
        STAMP(3);
        bf16x8 WO[28];
        {
            const v4u so = mk_srd(p.wo, D * D * 2);
            const unsigned ring = L_WST + w3 * 14336;
#pragma unroll
            for (int b = 0; b < 2; ++b) {
#pragma unroll
                for (int s = 0; s < 14; ++s) dma16(so, vw, (14 * b + s) * 16 * D * 2, lbase + ring + s * 1024);
                VMWAIT(0);
#pragma unroll
                for (int s = 0; s < 14; ++s) WO[14 * b + s] = mk8(trd(L + ring + s * 1024 + trofs), trd(L + ring + s * 1024 + trofs + 256));
                LGWAIT();
#pragma unroll
                for (int s = 0; s < 14; ++s) asm volatile("" : "+v"(WO[14 * b + s]));
            }
            if (jj < 28) {
                if (wave == 4) {
                    const v4u sg = mk_srd(p.wg, D * F * 2), su = mk_srd(p.wu, D * F * 2);
                    const unsigned vo = lane * (F * 2) + (xcd * 28 + jj) * 16;
#pragma unroll
                    for (int s = 0; s < 7; ++s) { dma16(sg, vo, s * 64 * F * 2, lbase + ring + s * 1024); dma16(su, vo, s * 64 * F * 2, lbase + ring + 7168 + s * 1024); }
                }
            } else if (xcd * 4 + jj - 28 < 28) {
                const v4u sd = mk_srd(p.wd, D * F * 2);
                const unsigned vo = (lane >> 1) * (D * 2) + (xcd * 4 + jj - 28) * 32 + (lane & 1) * 16;
#pragma unroll
                for (int s = 0; s < 14; ++s) dma16(sd, vo, (448 * w3 + 32 * s) * D * 2, lbase + ring + s * 1024);
            }
            VMWAIT(0);
        }
        STAMP(4);
        BAR();
        STAMP(5);
        {
            v4u r[11]; unsigned spins = 0; bool ok;
            do {
                ok = true; POLLMEM();
#pragma unroll
                for (int i = 0; i < 11; ++i) {
                    int e = w3 + 4 * i; if (e > 41) e = 41;
                    const int pm = (m + 1 + e / 14) & 3;
                    r[i] = BLOAD(WS_O + lane * 16, (cl * 4 + pm) * 14336 + (e % 14) * 1024);
                }
#pragma unroll
                for (int i = 0; i < 11; ++i) ok &= (r[i].y == tag && r[i].w == tag);
            } while (!__all(ok) && ++spins < SPIN_CAP);
#pragma unroll
            for (int i = 0; i < 11; ++i) {
                int e = w3 + 4 * i; if (e > 41) e = 41;
                const int pm = (m + 1 + e / 14) & 3;
                const int k4 = 28 * pm + 2 * (e % 14) + hh;
                *LDSP(v2u, L_XI + (k4 >> 2) * 1024 + (l31 + 32 * ((k4 >> 1) & 1)) * 16 + 8 * (k4 & 1)) = (v2u){r[i].x, r[i].z};
            }
        }
        STAMP(6);
        BAR();
        f32x16 acc = {};
#pragma unroll
        for (int s = 0; s < 28; ++s) acc = MFMA32(WO[s], as8(*LDSP(v4u, L_XI + s * 1024 + lane * 16)), acc);
        float hv[16];
        {
            float s1 = 0.f, s2 = 0.f, s3 = 0.f;
#pragma unroll
            for (int i = 0; i < 4; ++i) {
                const float xv[4] = {bflo(xr[i].x), bfhi(xr[i].x), bflo(xr[i].y), bfhi(xr[i].y)};
                const float wv[4] = {bflo(wrq[i].x), bfhi(wrq[i].x), bflo(wrq[i].y), bfhi(wrq[i].y)};
#pragma unroll
                for (int r = 0; r < 4; ++r) {
                    float h = rbf(xv[r] + rbf(acc[4 * i + r]));
                    if (i == 3 && hh) h = 0.f;
                    hv[4 * i + r] = h; s1 += h; s2 += h * h; s3 += h * wv[r];
                }
            }
            s1 = half_sum(s1); s2 = half_sum(s2); s3 = half_sum(s3);
            if (hh == 0) *LDSP(f32x4, L_PST + w3 * 512 + l31 * 16) = (f32x4){s1, s2, s3, 0.f};
        }
        STAMP(7);
        BAR();
        float mean_b, sd_b; unsigned key;
        {
            f32x4 a = *LDSP(f32x4, L_PST + l31 * 16);
#pragma unroll
            for (int i = 1; i < 4; ++i) a += *LDSP(f32x4, L_PST + i * 512 + l31 * 16);
            v4u r[3]; unsigned spins = 0; bool ok;
            do {
                ok = true; POLLMEM();
#pragma unroll
                for (int i = 0; i < 3; ++i) r[i] = BLOAD(WS_STAT + lane * 16, (cl * 4 + ((m + 1 + i) & 3)) * 1024);
#pragma unroll
                for (int i = 0; i < 3; ++i) ok &= (r[i].y == tag && r[i].w == tag);
            } while (!__all(ok) && ++spins < SPIN_CAP);
            STAMP(8);
            float pa[4], pb[4];
#pragma unroll
            for (int i = 0; i < 4; ++i) {
                const int rel = (i - m - 1) & 3;
                const float oa = hh ? a[2] : a[0], ob = a[1];
                const float va = rel == 0 ? __uint_as_float(r[0].x) : rel == 1 ? __uint_as_float(r[1].x) : rel == 2 ? __uint_as_float(r[2].x) : oa;
                const float vb = rel == 0 ? __uint_as_float(r[0].z) : rel == 1 ? __uint_as_float(r[1].z) : rel == 2 ? __uint_as_float(r[2].z) : ob;
                pa[i] = va; pb[i] = vb;
            }
            const float ta = (pa[0] + pa[1]) + (pa[2] + pa[3]), tb = (pb[0] + pb[1]) + (pb[2] + pb[3]);
            auto x = __builtin_amdgcn_permlane32_swap(__float_as_uint(ta), __float_as_uint(ta), false, false);
            auto y = __builtin_amdgcn_permlane32_swap(__float_as_uint(tb), __float_as_uint(tb), false, false);
            const float s1 = __uint_as_float(x[0]), s3 = __uint_as_float(x[1]), s2 = __uint_as_float(y[0]);
            const float mean = s1 * (1.f / D), var = fmaxf(s2 * (1.f / D) - mean * mean, 0.f);
            mean_b = rbf(mean); sd_b = rbf(sqrtf(rbf(rbf(var) + EPS)));
            const float rr = (s3 - mean_b * wsum) / sd_b;
            const unsigned rb = __builtin_bit_cast(uint16_t, (__bf16)rr);
            key = ((rb ^ ((rb & 0x8000u) ? 0xffffu : 0x8000u)) << 16) | (unsigned)(2047 - (t0 + l31));
        }
        const unsigned k0 = umax32(key), k1 = umax32(key == k0 ? 0u : key);
        const bool cand = key == k0 || key == k1;
        if (m == 0 && wave == 4 && lane == 0) BSTORE(((v4u){k0, tag, k1, tag}), WS_CAND, cl * 128);
        v2u ov[4];
        {
            const float rsd = 1.f / sd_b;
#pragma unroll
            for (int i = 0; i < 4; ++i) {
                float n[4];
#pragma unroll
                for (int r = 0; r < 4; ++r) n[r] = rbf(rbf(hv[4 * i + r] - mean_b) * rsd);
                ov[i] = (v2u){pk(hv[4 * i] + n[0], hv[4 * i + 1] + n[1]), pk(hv[4 * i + 2] + n[2], hv[4 * i + 3] + n[3])};
                if (cand && (i < 3 || hh == 0)) {
                    const unsigned ro = WS_ROW + (cl * 2 + (key == k1)) * 3584 + (28 * m + 7 * w3 + 2 * i + hh) * 16;
                    BSTORE(((v4u){pk(n[0], n[1]), tag, pk(n[2], n[3]), tag}), ro, 0);
                    BSTORE(((v4u){pk(hv[4 * i], hv[4 * i + 1]), tag, pk(hv[4 * i + 2], hv[4 * i + 3]), tag}), ro + 1792, 0);
                }
            }
        }
        STAMP(9);
        GAS v2u* orow = (GAS v2u*)(p.out + (size_t)(t0 + l31) * D + 112 * m + 28 * w3 + 4 * hh);
        if (!cand) {
#pragma unroll
            for (int i = 0; i < 4; ++i) if (i < 3 || hh == 0) orow[2 * i] = ov[i];
        }
        BAR();
        const v2u win = *LDSP(v2u, L_WIN);
        if (cand && key != win.x && key != win.y) {
#pragma unroll
            for (int i = 0; i < 4; ++i) if (i < 3 || hh == 0) orow[2 * i] = ov[i];
        }
        STAMP(12);
        STAMP_FLUSH();
    }
}
}

extern "C" void kernel_launch(void* const* d_in, const int* in_sizes, int n_in, void* d_out, int out_size, void* d_ws, size_t ws_size, hipStream_t stream) {
    Params p;
    p.x = (const uint16_t*)d_in[0]; p.wq = (const uint16_t*)d_in[1]; p.wk = (const uint16_t*)d_in[2]; p.wv = (const uint16_t*)d_in[3]; p.wo = (const uint16_t*)d_in[4];
    p.wr = (const uint16_t*)d_in[6]; p.wg = (const uint16_t*)d_in[7]; p.wu = (const uint16_t*)d_in[8]; p.wd = (const uint16_t*)d_in[9];
    p.out = (uint16_t*)d_out; p.ws = (unsigned char*)d_ws;
    fused_kernel<<<NWG, NTHR, 0, stream>>>(p);
}
```

```cpp
#include <hip/hip_runtime.h>
#include <stdint.h>

#define GAS __attribute__((address_space(1)))
#define LAS __attribute__((address_space(3)))
#define CAS __attribute__((address_space(4)))
typedef unsigned v4u __attribute__((ext_vector_type(4)));
typedef unsigned v2u __attribute__((ext_vector_type(2)));
typedef float f32x16 __attribute__((ext_vector_type(16)));
typedef float f32x8 __attribute__((ext_vector_type(8)));
typedef float f32x4 __attribute__((ext_vector_type(4)));
typedef float f32x2 __attribute__((ext_vector_type(2)));
typedef __bf16 bf16x2 __attribute__((ext_vector_type(2)));
typedef __bf16 bf16x8 __attribute__((ext_vector_type(8)));
typedef __bf16 bf16x16 __attribute__((ext_vector_type(16)));
typedef short v4i16 __attribute__((ext_vector_type(4)));
typedef GAS unsigned gu32;
typedef GAS unsigned long long gu64;
#define RLX_AGENT __ATOMIC_RELAXED, __HIP_MEMORY_SCOPE_AGENT

namespace {
constexpr int S = 2048, D = 448, DH = 28, F = 1792;
constexpr int NWG = 256, NTHR = 512;
constexpr float QSCALE = 0.18898223650461361f * 1.4426950408889634f;
constexpr float EPS = 1e-5f;
constexpr unsigned SPIN_CAP = 1u << 18;

constexpr unsigned WS_EPOCH = 0;
constexpr unsigned WS_FLAG = 4096;
constexpr unsigned WS_CAND = WS_FLAG + 16384;
constexpr unsigned WS_ACT = WS_CAND + 8192;
constexpr unsigned WS_KV = 65536;
constexpr unsigned WS_STAT = WS_KV + 262144;
constexpr unsigned WS_ROW = WS_STAT + 131072;
constexpr unsigned WS_O = WS_ROW + 128 * 2048;
constexpr unsigned WS_END = WS_O + 256 * 8192;
constexpr unsigned WS_DBG = 8u << 20;

constexpr unsigned L_ST = 0;
constexpr unsigned STB = 28672;
constexpr unsigned L_WKV = L_ST + 4 * STB;
constexpr unsigned L_KVP = L_WKV + 26624;
constexpr unsigned L_QI = L_KVP + 4 * 2304;
constexpr unsigned L_SP = L_QI + 8192;
constexpr unsigned L_CSQ = L_SP + 1024;
constexpr unsigned L_CNT = L_CSQ + 2048;
constexpr unsigned LDS_BYTES = L_CNT + 64;
constexpr unsigned L_RED = 28672, L_OI = L_RED + 49152, L_FA = L_WKV, L_FS = 57344;
static_assert(LDS_BYTES <= 163840 && L_OI + 32768 <= L_WKV, "lds");
enum { C_Q = 0, C_KV = 1, C_ST = 2, C_FB = 3, C_WINOK = 4, C_WO = 5, C_W0 = 6, C_W1 = 7, C_CO = 8 };

struct Params {
    const uint16_t* x; const uint16_t* wq; const uint16_t* wk; const uint16_t* wv; const uint16_t* wo;
    const uint16_t* wr; const uint16_t* wg; const uint16_t* wu; const uint16_t* wd;
    uint16_t* out; unsigned char* ws;
};

#define SBAR() __builtin_amdgcn_sched_barrier(0)
#define BAR() asm volatile("s_waitcnt lgkmcnt(0)\n\ts_barrier" ::: "memory")
#define VMWAIT(n) asm volatile("s_waitcnt vmcnt(" #n ")" ::: "memory")
#define LGWAIT() asm volatile("s_waitcnt lgkmcnt(0)" ::: "memory")

__device__ __forceinline__ v4u mk_srd(const void* p, unsigned bytes) {
    const unsigned long long a = (unsigned long long)p;
    v4u r; r.x = (unsigned)a; r.y = (unsigned)(a >> 32) & 0xffffu; r.z = bytes; r.w = 0x00020000u; return r;
}
__device__ __forceinline__ void dma16(v4u srd, unsigned voff, unsigned soff, unsigned ldsaddr) {
    asm volatile("s_mov_b32 m0, %0\n\ts_nop 0\n\tbuffer_load_dwordx4 %1, %2, %3 offen lds" :: "s"(ldsaddr), "v"(voff), "s"(srd), "s"(soff) : "memory");
}
template <int IMM> __device__ __forceinline__ v4u aload16(v4u srd, unsigned voff, unsigned soff) {
    v4u r; asm volatile("buffer_load_dwordx4 %0, %1, %2, %3 offen offset:%4" : "=v"(r) : "v"(voff), "s"(srd), "s"(soff), "i"(IMM) : "memory"); return r;
}
__device__ __forceinline__ unsigned pk(float lo, float hi) {
    f32x2 v = {lo, hi}; return __builtin_bit_cast(unsigned, __builtin_convertvector(v, bf16x2));
}
__device__ __forceinline__ float bflo(unsigned u) { return __uint_as_float(u << 16); }
__device__ __forceinline__ float bfhi(unsigned u) { return __uint_as_float(u & 0xffff0000u); }
__device__ __forceinline__ float rbf(float f) { return (float)(__bf16)f; }
__device__ __forceinline__ v2u trd(LAS char* p) { return __builtin_bit_cast(v2u, __builtin_amdgcn_ds_read_tr16_b64_v4i16((LAS v4i16*)p)); }
__device__ __forceinline__ bf16x8 mk8(v2u a, v2u b) { v4u r = {a.x, a.y, b.x, b.y}; return __builtin_bit_cast(bf16x8, r); }
__device__ __forceinline__ bf16x8 as8(v4u a) { return __builtin_bit_cast(bf16x8, a); }
__device__ __forceinline__ float dot2(unsigned a, unsigned b, float c) { return __builtin_amdgcn_fdot2_f32_bf16(__builtin_bit_cast(bf16x2, a), __builtin_bit_cast(bf16x2, b), c, false); }
template <int CTRL> __device__ __forceinline__ float dppf(float x) { return __builtin_bit_cast(float, __builtin_amdgcn_mov_dpp(__builtin_bit_cast(int, x), CTRL, 0xf, 0xf, true)); }
template <int CTRL> __device__ __forceinline__ unsigned dppu(unsigned x) { return (unsigned)__builtin_amdgcn_mov_dpp((int)x, CTRL, 0xf, 0xf, true); }
constexpr int XOR1 = 0xB1, XOR2 = 0x4E, XOR7 = 0x141, ROR8 = 0x128, MIR15 = 0x140;
__device__ __forceinline__ float half_sum(float v) { auto r = __builtin_amdgcn_permlane32_swap(__float_as_uint(v), __float_as_uint(v), false, false); return __uint_as_float(r[0]) + __uint_as_float(r[1]); }
__device__ __forceinline__ unsigned umax32(unsigned v) {
    v = max(v, dppu<XOR1>(v)); v = max(v, dppu<XOR2>(v)); v = max(v, dppu<XOR7>(v)); v = max(v, dppu<MIR15>(v));
    auto r = __builtin_amdgcn_permlane16_swap(v, v, false, false); return max(r[0], r[1]);
}
__device__ __forceinline__ unsigned umax64(unsigned v) { v = umax32(v); auto r = __builtin_amdgcn_permlane32_swap(v, v, false, false); return max(r[0], r[1]); }
#define MFMA32(a, b, c) __builtin_amdgcn_mfma_f32_32x32x16_bf16(a, b, c, 0, 0, 0)
#define MFMA16(a, b, c) __builtin_amdgcn_mfma_f32_16x16x32_bf16(a, b, c, 0, 0, 0)
__device__ __forceinline__ f32x16 smf_lo(bf16x8 v, const f32x16& p, f32x16 o, int idx) {
    f32x8 h = __builtin_shufflevector(p, p, 0, 1, 2, 3, 4, 5, 6, 7);
    return __builtin_amdgcn_smfmac_f32_32x32x32_bf16(v, __builtin_bit_cast(bf16x16, h), o, idx, 0, 0);
}
__device__ __forceinline__ f32x16 smf_hi(bf16x8 v, const f32x16& p, f32x16 o, int idx) {
    f32x8 h = __builtin_shufflevector(p, p, 8, 9, 10, 11, 12, 13, 14, 15);
    return __builtin_amdgcn_smfmac_f32_32x32x32_bf16(v, __builtin_bit_cast(bf16x16, h), o, idx, 0, 0);
}
#define LDSP(T, off) ((LAS T*)(L + (off)))
#define BLOAD(off, soff) __builtin_amdgcn_raw_buffer_load_b128(wsr, (off), (soff), 16)
#define BSTORE(v, off, soff) __builtin_amdgcn_raw_buffer_store_b128((v), wsr, (off), (soff), 16)

#define STAMP(i) do {} while (0)
#define STAMPC(i) do {} while (0)
#define STAMP_DECL do {} while (0)
#define STAMP_FLUSH() do {} while (0)
#define POLLMEM() asm volatile("" ::: "memory")

__device__ __forceinline__ unsigned x3(unsigned a, unsigned b, unsigned c) { return __builtin_amdgcn_bitop3_b32(a, b, c, 0x96); }
__device__ __forceinline__ unsigned x4(v4u a) { return x3(a.x, a.y, a.z) ^ a.w; }
__device__ __forceinline__ unsigned x8(v4u a, v4u b) { return x3(x3(a.x, a.y, a.z), x3(a.w, b.x, b.y), x3(b.z, b.w, 0u)); }
__device__ __forceinline__ float frcp(float x) { return __builtin_amdgcn_rcpf(x); }
__device__ __forceinline__ float fsq(float x) { return __builtin_amdgcn_sqrtf(x); }
__device__ __forceinline__ float row_sum(float v) { auto r = __builtin_amdgcn_permlane16_swap(__float_as_uint(v), __float_as_uint(v), false, false); return __uint_as_float(r[0]) + __uint_as_float(r[1]); }
#define CNT(i) ((LAS unsigned*)(L + L_CNT) + (i))
#define CNT_ADD(i) __hip_atomic_fetch_add(CNT(i), 1u, __ATOMIC_RELAXED, __HIP_MEMORY_SCOPE_WORKGROUP)
#define CNT_LD(i) __hip_atomic_load(CNT(i), __ATOMIC_RELAXED, __HIP_MEMORY_SCOPE_WORKGROUP)
#define ONES2 0x3f803f80u

extern "C" __global__ __launch_bounds__(NTHR, 2) void fused_kernel(Params p) {
    __shared__ __attribute__((aligned(16))) char lds_[LDS_BYTES];
    LAS char* L = (LAS char*)lds_;
    const unsigned lbase = (unsigned)(uintptr_t)L;
    const int g = blockIdx.x, xcd = g & 7, jj = g >> 3, cl = xcd * 8 + (jj >> 2), m = jj & 3, t0 = cl * 32;
    const int tid = threadIdx.x, lane = tid & 63, wave = __builtin_amdgcn_readfirstlane(tid >> 6);
    const int l31 = lane & 31, hh = lane >> 5, l15 = lane & 15, gq = lane >> 4, q4 = (lane & 15) >> 2;
    const int w3 = wave & 3;
    STAMP_DECL;
    STAMP(0);
    unsigned ctouch = 0;
    if (wave == 3) {
        unsigned long long base; unsigned len, len2;
        asm volatile("s_getpc_b64 %0\n.Lfk_here:\n\ts_mov_b32 %1, .Lfk_end_a-.Lfk_here\n\ts_mov_b32 %2, .Lfk_end_b-.Lfk_here" : "=s"(base), "=s"(len), "=s"(len2));
        len = max(len, len2);
        unsigned off = (unsigned)((jj & 3) * 64 + lane) * 128u;
        off = off < len - 4u ? off : 0u;
        asm volatile("global_load_dword %0, %1, %2" : "=v"(ctouch) : "v"(off), "s"(base) : "memory");
    }
    if (tid < 16) *CNT(tid) = 0u;
    BAR();
    const unsigned tag = *(const CAS unsigned*)(p.ws + WS_EPOCH) + 1u;
    const __amdgpu_buffer_rsrc_t wsr = __builtin_amdgcn_make_buffer_rsrc(p.ws, 0, WS_END, 0x00020000);
    const unsigned vchunk = (l15 - 4 * gq) & 15;
    const unsigned vw = gq * (D * 2) + 224 * m + (vchunk < 14 ? vchunk : 13) * 16;
    const bool isA = jj < 28;
    const int fb = xcd * 4 + (jj - 28);
    const bool isB = !isA && fb < 28;

    if (wave < 4) {
        const unsigned st = L_ST + w3 * STB;
        v4u XF[7]; unsigned tq;
        const v4u sq = mk_srd(p.wq, D * D * 2);
        {
            const v4u sx = mk_srd(p.x + (size_t)t0 * D, 32 * D * 2);
            { const unsigned ln = jj * 98 + 25 * w3 + lane;
              asm volatile("buffer_load_dword %0, %1, %2, 0 offen" : "=v"(tq) : "v"(lane < 25 ? ln * 128 : 0x7fffffffu), "s"(sq) : "memory"); }
            const unsigned vx = gq * (D * 2) + 224 * w3 + (vchunk < 14 ? vchunk : 13) * 16;
#pragma unroll
            for (int i = 0; i < 8; ++i) dma16(sx, vx, 4 * i * D * 2, lbase + st + (20 + i) * 1024);
        }
#define QDMA(s) do { _Pragma("unroll") for (int i_ = 4 * (s); i_ < 4 * (s) + 4; ++i_) dma16(sq, vw, (112 * w3 + 4 * i_) * D * 2, lbase + st + i_ * 1024); } while (0)
        QDMA(0); QDMA(1); QDMA(2); QDMA(3); QDMA(4);
        f32x16 acc[4] = {};
        float cs[4] = {0.f, 0.f, 0.f, 0.f}, s1 = 0.f, s2 = 0.f;
        unsigned tra[4];
#pragma unroll
        for (int i = 0; i < 4; ++i) { const unsigned b = 56 * ((w3 + 1 + i) & 3) + 32 * (gq & 1) + 8 * (lane & 3);
            tra[i] = st + (8 * hh + q4) * 256 + ((((b >> 4) + 4 * q4) & 15) << 4) + (b & 8); }
        v2u WA[2][8];
#define QRD(s) do { _Pragma("unroll") for (int i_ = 0; i_ < 4; ++i_) { WA[(s) & 1][2 * i_] = trd(L + tra[i_] + (s) * 4096); WA[(s) & 1][2 * i_ + 1] = trd(L + tra[i_] + (s) * 4096 + 1024); } } while (0)
#define QMM(s) do { _Pragma("unroll") for (int i_ = 0; i_ < 4; ++i_) { const v2u a0 = WA[(s) & 1][2 * i_], a1 = WA[(s) & 1][2 * i_ + 1]; \
            acc[i_] = MFMA32(mk8(a0, a1), as8(XF[s]), acc[i_]); \
            cs[i_] = dot2(a0.x, ONES2, cs[i_]); s1 = dot2(XF[s][i_], ONES2, s1); cs[i_] = dot2(a0.y, ONES2, cs[i_]); s2 = dot2(XF[s][i_], XF[s][i_], s2); \
            cs[i_] = dot2(a1.x, ONES2, cs[i_]); cs[i_] = dot2(a1.y, ONES2, cs[i_]); } SBAR(); } while (0)
        VMWAIT(20);
        asm volatile("" : "+v"(tq), "+v"(ctouch));
        STAMP(1);
#pragma unroll
        for (int s = 0; s < 7; ++s) XF[s] = *LDSP(v4u, st + 20480 + l31 * 256 + (((2 * s + hh + 4 * (l31 & 3)) & 15) << 4));
        asm volatile("s_waitcnt lgkmcnt(0)" : "+v"(XF[0]), "+v"(XF[1]), "+v"(XF[2]), "+v"(XF[3]), "+v"(XF[4]), "+v"(XF[5]), "+v"(XF[6]) :: "memory");
        QDMA(5); QDMA(6);
        VMWAIT(24); QRD(0);
        VMWAIT(20); QRD(1); SBAR(); QMM(0);
        VMWAIT(16); QRD(2); SBAR(); QMM(1);
        VMWAIT(12); QRD(3); SBAR(); QMM(2);
        VMWAIT(8); QRD(4); SBAR(); QMM(3);
        VMWAIT(4); QRD(5); SBAR(); QMM(4);
        VMWAIT(0); QRD(6); SBAR(); QMM(5);
        QMM(6);
#undef QDMA
#undef QRD
#undef QMM
        STAMP(2);
        {
#pragma unroll
            for (int i = 0; i < 3; ++i)
#pragma unroll
                for (int k = 0; k < 4; ++k) *LDSP(f32x4, st + i * 4096 + k * 1024 + lane * 16) = (f32x4){acc[i][4 * k], acc[i][4 * k + 1], acc[i][4 * k + 2], acc[i][4 * k + 3]};
            s1 = half_sum(s1); s2 = half_sum(s2);
#pragma unroll
            for (int i = 0; i < 4; ++i) cs[i] = half_sum(cs[i]);
            if (hh == 0) {
                *LDSP(f32x2, L_SP + w3 * 256 + l31 * 8) = (f32x2){s1, s2};
#pragma unroll
                for (int i = 0; i < 4; ++i) *LDSP(float, L_CSQ + (((w3 + 1 + i) & 3) * 4 + w3) * 128 + l31 * 4) = cs[i];
            }
            LGWAIT();
            if (lane == 0) CNT_ADD(C_Q);
            { unsigned spins = 0; while (CNT_LD(C_Q) < 4u && ++spins < SPIN_CAP) {} POLLMEM(); }
            f32x4 v[12], c4[16]; f32x2 sp[4];
#pragma unroll
            for (int i = 0; i < 3; ++i)
#pragma unroll
                for (int k = 0; k < 4; ++k) v[4 * i + k] = *LDSP(f32x4, L_ST + ((w3 + 3 - i) & 3) * STB + i * 4096 + k * 1024 + lane * 16);
#pragma unroll
            for (int i = 0; i < 4; ++i) {
                sp[i] = *LDSP(f32x2, L_SP + i * 256 + l31 * 8);
#pragma unroll
                for (int k = 0; k < 4; ++k) c4[4 * i + k] = *LDSP(f32x4, L_CSQ + (w3 * 4 + i) * 128 + (8 * k + 4 * hh) * 4);
            }
            const f32x2 ss = (sp[0] + sp[1]) + (sp[2] + sp[3]);
            const float mean = ss.x * (1.f / D), var = fmaxf(ss.y * (1.f / D) - mean * mean, 0.f);
            const float mb = rbf(mean), sc = frcp(rbf(fsq(rbf(rbf(var) + EPS)))) * QSCALE;
            unsigned qp[8];
#pragma unroll
            for (int k = 0; k < 4; ++k) {
                const f32x4 a = (f32x4){acc[3][4 * k], acc[3][4 * k + 1], acc[3][4 * k + 2], acc[3][4 * k + 3]} + v[k] + (v[4 + k] + v[8 + k]);
                const f32x4 c = (c4[k] + c4[4 + k]) + (c4[8 + k] + c4[12 + k]);
                const f32x4 q = (a - mb * c) * sc;
                qp[2 * k] = pk(q[0], q[1]); qp[2 * k + 1] = pk(q[2], q[3]);
            }
            if (hh) { qp[6] = 0u; qp[7] = 0u; }
            *LDSP(v4u, L_QI + w3 * 2048 + lane * 16) = (v4u){qp[0], qp[1], qp[4], qp[5]};
            *LDSP(v4u, L_QI + w3 * 2048 + 1024 + lane * 16) = (v4u){qp[2], qp[3], qp[6], qp[7]};
        }
        STAMP(3);
        BAR();
        STAMP(4);
        const int rot = (g >> 2) & 15;
        f32x16 O[4] = {}, Sb[4];
        v4u KB[4][2], VB[4][2];
        const unsigned kvo = lane * 16;
        const int idx = (int)0xDDDDDDDDu;
        const v4u skv = mk_srd(p.ws + WS_KV, 262144);
        unsigned so = (unsigned)(((4 * rot + w3) & 63) << 12);
#define KVLD(dst, imm) asm volatile("buffer_load_dwordx4 %0, %1, %2, %3 offen offset:" #imm " sc1" : "=v"(dst) : "v"(kvo), "s"(skv), "s"(so) : "memory")
#define LDK(b) do { KVLD(KB[b][0], 0); KVLD(KB[b][1], 1024); } while (0)
#define LDV(b) do { KVLD(VB[b][0], 2048); KVLD(VB[b][1], 3072); so = (so + 16384u) & 0x3ffffu; } while (0)
        LDK(0); LDV(0); LDK(1); LDV(1); LDK(2); LDV(2);
        bf16x8 Q[4][2];
#pragma unroll
        for (int i = 0; i < 4; ++i) {
            const unsigned qa = L_QI + ((w3 + 1 + i) & 3) * 2048 + lane * 16;
            Q[i][0] = as8(*LDSP(v4u, qa)); Q[i][1] = as8(*LDSP(v4u, qa + 1024));
        }
        asm volatile("" : "+v"(Q[0][0]), "+v"(Q[0][1]), "+v"(Q[1][0]), "+v"(Q[1][1]), "+v"(Q[2][0]), "+v"(Q[2][1]), "+v"(Q[3][0]), "+v"(Q[3][1]));
        VB[3][1] = (v4u){0u, 0u, 0u, 0u};
        Sb[3] = (f32x16){};
        asm volatile("s_waitcnt vmcnt(10)" : "+v"(KB[0][0]), "+v"(KB[0][1]) :: "memory");
        Sb[0] = MFMA32(as8(KB[0][0]), Q[0][0], (f32x16){});
        Sb[0] = MFMA32(as8(KB[0][1]), Q[0][1], Sb[0]);
#define EXP4(X, o) do { X[o] = __builtin_amdgcn_exp2f(X[o]); X[o + 1] = __builtin_amdgcn_exp2f(X[o + 1]); X[o + 2] = __builtin_amdgcn_exp2f(X[o + 2]); X[o + 3] = __builtin_amdgcn_exp2f(X[o + 3]); } while (0)
#define UNIT(b, q, nb, pb) do { \
            Sb[((q) + 1) & 3] = MFMA32(as8(KB[nb][0]), Q[((q) + 1) & 3][0], (f32x16){}); SBAR(); EXP4(Sb[q], 0); SBAR(); \
            Sb[((q) + 1) & 3] = MFMA32(as8(KB[nb][1]), Q[((q) + 1) & 3][1], Sb[((q) + 1) & 3]); SBAR(); EXP4(Sb[q], 4); SBAR(); \
            O[((q) + 3) & 3] = smf_hi(as8(VB[pb][1]), Sb[((q) + 3) & 3], O[((q) + 3) & 3], idx); SBAR(); EXP4(Sb[q], 8); SBAR(); \
            O[q] = smf_lo(as8(VB[b][0]), Sb[q], O[q], idx); SBAR(); EXP4(Sb[q], 12); SBAR(); } while (0)
#define REDW(i) do { const unsigned ra_ = L_RED + (((w3 + 1 + (i)) & 3) * 3 + (i)) * 4096 + lane * 16; \
            _Pragma("unroll") for (int k_ = 0; k_ < 4; ++k_) *LDSP(f32x4, ra_ + k_ * 1024) = (f32x4){O[i][4 * k_], O[i][4 * k_ + 1], O[i][4 * k_ + 2], O[i][4 * k_ + 3]}; } while (0)
#define TILE(b, LAST) do { \
            asm volatile("s_waitcnt vmcnt(6)" : "+v"(VB[b][0]), "+v"(VB[b][1]), "+v"(KB[((b) + 1) & 3][0]), "+v"(KB[((b) + 1) & 3][1]) :: "memory"); \
            LDK(((b) + 3) & 3); SBAR(); \
            UNIT(b, 0, b, ((b) + 3) & 3); \
            LDV(((b) + 3) & 3); SBAR(); \
            UNIT(b, 1, b, b); UNIT(b, 2, b, b); \
            if (LAST) { REDW(0); SBAR(); } \
            UNIT(b, 3, ((b) + 1) & 3, b); } while (0)
        STAMPC(14); STAMP(13);
#pragma unroll 1
        for (int it = 0; it < 16; it += 4) { TILE(0, false); TILE(1, false); TILE(2, false); TILE(3, it == 12); }
        REDW(1);
        O[3] = smf_hi(as8(VB[3][1]), Sb[3], O[3], idx);
        REDW(2);
        STAMPC(15);
        asm volatile("s_waitcnt vmcnt(0)" : "+v"(KB[0][0]), "+v"(KB[0][1]), "+v"(KB[1][0]), "+v"(KB[1][1]), "+v"(KB[2][0]), "+v"(KB[2][1]), "+v"(VB[0][0]), "+v"(VB[0][1]), "+v"(VB[1][0]), "+v"(VB[1][1]), "+v"(VB[2][0]), "+v"(VB[2][1]) :: "memory");
#undef UNIT
#undef TILE
#undef EXP4
#undef LDK
#undef KVLD
#undef LDV
#undef REDW
        STAMP(5);
        BAR();
        const unsigned salt = (tag * 0x9E3779B1u) | 1u;
        {
            f32x4 v[12];
#pragma unroll
            for (int s = 0; s < 3; ++s)
#pragma unroll
                for (int i = 0; i < 4; ++i) v[4 * s + i] = *LDSP(f32x4, L_RED + (w3 * 3 + s) * 4096 + i * 1024 + lane * 16);
            f32x16 o = O[3];
#pragma unroll
            for (int s = 0; s < 3; ++s)
#pragma unroll
                for (int i = 0; i < 4; ++i) { o[4 * i] += v[4 * s + i][0]; o[4 * i + 1] += v[4 * s + i][1]; o[4 * i + 2] += v[4 * s + i][2]; o[4 * i + 3] += v[4 * s + i][3]; }
            auto r = __builtin_amdgcn_permlane32_swap(__float_as_uint(o[12]), __float_as_uint(o[12]), false, false);
            const float rl = frcp(__uint_as_float(r[1]));
            unsigned d[8];
#pragma unroll
            for (int i = 0; i < 8; ++i) d[i] = pk(o[2 * i] * rl, o[2 * i + 1] * rl);
            auto sw = __builtin_amdgcn_permlane32_swap(d[6], d[7], false, false);
            v4u ra = {d[0], d[1], d[2], d[3]}, rb = {d[4], d[5], sw[0], 0u};
            *LDSP(v4u, L_OI + m * 8192 + (2 * w3) * 1024 + lane * 16) = ra;
            *LDSP(v4u, L_OI + m * 8192 + (2 * w3 + 1) * 1024 + lane * 16) = rb;
            rb.w = x8(ra, rb) ^ salt;
            BSTORE(ra, WS_O + lane * 16, (cl * 4 + m) * 8192 + w3 * 2048);
            BSTORE(rb, WS_O + 1024 + lane * 16, (cl * 4 + m) * 8192 + w3 * 2048);
        }
        STAMP(6);
        BAR();
        {
            v4u ra[3], rb[3]; unsigned spins = 0; bool ok;
            do {
                ok = true; POLLMEM();
#pragma unroll
                for (int i = 0; i < 3; ++i) {
                    const unsigned so = (cl * 4 + ((m + 1 + i) & 3)) * 8192 + w3 * 2048;
                    ra[i] = BLOAD(WS_O + lane * 16, so); rb[i] = BLOAD(WS_O + 1024 + lane * 16, so);
                }
#pragma unroll
                for (int i = 0; i < 3; ++i) ok &= x8(ra[i], rb[i]) == salt;
            } while (!__all(ok) && ++spins < SPIN_CAP);
#pragma unroll
            for (int i = 0; i < 3; ++i) {
                const unsigned oa = L_OI + ((m + 1 + i) & 3) * 8192 + (2 * w3) * 1024 + lane * 16;
                rb[i].w = 0u;
                *LDSP(v4u, oa) = ra[i]; *LDSP(v4u, oa + 1024) = rb[i];
            }
        }
        STAMP(7);
        BAR();
        bf16x8 WF[16];
        if (isA && wave == 0) {
            const unsigned wb = L_FA + ((lane >> 1) & 1) * 7168 + 8 * (lane & 1);
            const unsigned r0 = 16 * (gq & 1) + 4 * (gq >> 1) + q4, r1 = (gq & 1) ? 24 + 2 * (gq >> 1) + (q4 & 1) : 8 + 4 * (gq >> 1) + q4;
#pragma unroll
            for (int s = 0; s < 16; ++s) WF[s] = mk8(trd(L + wb + r0 * 16 + s * 448), trd(L + wb + r1 * 16 + s * 448));
        } else if (isB) {
            { unsigned spins = 0; while (CNT_LD(C_CO) < 4u && ++spins < SPIN_CAP) __builtin_amdgcn_s_sleep(4); POLLMEM(); }
            const v4u sd = mk_srd(p.wd, D * F * 2);
            const unsigned vo = (lane >> 1) * (D * 2) + fb * 32 + (lane & 1) * 16;
#pragma unroll
            for (int s = 0; s < 14; ++s) dma16(sd, vo, (448 * w3 + 32 * s) * D * 2, lbase + L_ST + w3 * 14336 + s * 1024);
            VMWAIT(0);
            const unsigned wb = L_ST + w3 * 14336 + (8 * gq + q4) * 32 + 8 * (lane & 3);
#pragma unroll
            for (int s = 0; s < 14; ++s) WF[s] = mk8(trd(L + wb + s * 1024), trd(L + wb + s * 1024 + 128));
        }
        if (wave == 0) {
            v4u cv; unsigned spins = 0;
            do { POLLMEM(); cv = BLOAD(WS_CAND + lane * 128, 0); } while (!__all(cv.y == tag && cv.w == tag) && ++spins < SPIN_CAP);
            STAMP(8);
            const unsigned w0 = umax64(cv.x);
            const unsigned w1 = umax64(cv.x == w0 ? cv.z : cv.x);
            const unsigned long long b0 = __ballot(cv.x == w0), b1a = __ballot(cv.x == w1), b1b = __ballot(cv.z == w1);
            const int c0 = 2 * (int)__builtin_ctzll(b0);
            const int c1 = b1a ? 2 * (int)__builtin_ctzll(b1a) : 2 * (int)__builtin_ctzll(b1b | (1ull << 63)) + 1;
            const unsigned ro = WS_ROW + (hh ? c1 : c0) * 2048 + l31 * 32;
            if (isA) {
                v4u ra, rb; spins = 0;
                do { POLLMEM(); ra = BLOAD(ro, 0); rb = BLOAD(ro + 16, 0); } while (!__all(x8(ra, rb) == salt) && ++spins < SPIN_CAP);
                STAMP(9);
                rb.w = 0u;
                *LDSP(v4u, L_FS + lane * 32) = ra; *LDSP(v4u, L_FS + lane * 32 + 16) = rb;
                LGWAIT();
                v4u xf[16];
#pragma unroll
                for (int s = 0; s < 16; ++s) xf[s] = *LDSP(v4u, L_FS + (lane & 1) * 1024 + s * 64 + gq * 16);
                f32x4 y = {}, y2 = {};
#pragma unroll
                for (int s = 0; s < 16; s += 2) { y = MFMA16(as8(xf[s]), WF[s], y); y2 = MFMA16(as8(xf[s + 1]), WF[s + 1], y2); }
                y += y2;
                const float u0 = dppf<ROR8>(y[0]), u1 = dppf<ROR8>(y[1]);
                const float g0 = rbf(y[0]), g1 = rbf(y[1]);
                const float a0 = rbf(g0 * frcp(1.f + __expf(-g0))) * rbf(u0), a1 = rbf(g1 * frcp(1.f + __expf(-g1))) * rbf(u1);
                const float n0 = dppf<XOR1>(a0), n1 = dppf<XOR1>(a1);
                const unsigned pay = (lane & 1) ? pk(n1, a1) : pk(a0, n0);
                if (lane < 8) __hip_atomic_store((gu64*)(p.ws + WS_ACT + (xcd * 28 + jj) * 64 + (lane & 1) * 32 + (lane >> 1) * 8), ((unsigned long long)tag << 32) | pay, RLX_AGENT);
                STAMP(10);
            }
            if (lane == 0) { *CNT(C_W0) = w0; *CNT(C_W1) = w1; }
            LGWAIT();
            if (lane == 0) __hip_atomic_store(CNT(C_WINOK), 1u, __ATOMIC_RELAXED, __HIP_MEMORY_SCOPE_WORKGROUP);
            if (isB) {
                if (fb == 0 && lane == 0) __hip_atomic_store((gu32*)(p.ws + WS_EPOCH), tag, RLX_AGENT);
                v4u ra, rb, rc, rd; spins = 0;
                do { POLLMEM(); ra = BLOAD(ro, 0); rb = BLOAD(ro + 16, 0); rc = BLOAD(ro + 1024, 0); rd = BLOAD(ro + 1040, 0); }
                while (!__all(x8(ra, rb) == salt && x8(rc, rd) == salt) && ++spins < SPIN_CAP);
                *LDSP(v4u, L_FS + 8192 + lane * 32) = ra; *LDSP(v4u, L_FS + 8192 + lane * 32 + 16) = rb;
                *LDSP(v4u, L_FS + 10240 + lane * 32) = rc; *LDSP(v4u, L_FS + 10240 + lane * 32 + 16) = rd;
            }
        }
        if (isB) {
            v4u r[4]; unsigned spins = 0; bool ok;
            do {
                ok = true; POLLMEM();
#pragma unroll
                for (int i = 0; i < 4; ++i) { const int u = lane + 64 * i; r[i] = BLOAD(WS_ACT + (u < 224 ? u : 0) * 16, w3 * 3584); }
#pragma unroll
                for (int i = 0; i < 4; ++i) ok &= (r[i].y == tag && r[i].w == tag);
            } while (!__all(ok) && ++spins < SPIN_CAP);
            STAMP(11);
#pragma unroll
            for (int i = 0; i < 4; ++i) { const int u = lane + 64 * i; if (u < 224) *LDSP(v2u, L_FS + w3 * 1792 + u * 8) = (v2u){r[i].x, r[i].z}; }
            LGWAIT();
            v4u xf[14];
#pragma unroll
            for (int s = 0; s < 14; ++s) xf[s] = *LDSP(v4u, L_FS + w3 * 1792 + (4 * s + gq) * 32 + (lane & 1) * 16);
            f32x4 y = {}, y2 = {};
#pragma unroll
            for (int s = 0; s < 14; s += 2) { y = MFMA16(as8(xf[s]), WF[s], y); y2 = MFMA16(as8(xf[s + 1]), WF[s + 1], y2); }
            y += y2;
            if (lane < 16) *LDSP(f32x2, L_FS + 7168 + w3 * 128 + lane * 8) = (f32x2){y[0], y[1]};
            LGWAIT();
            unsigned arr = 0;
            if (lane == 0) arr = CNT_ADD(C_FB);
            if (__builtin_amdgcn_readfirstlane(arr) == 3u && lane < 32) {
                POLLMEM();
                float yy = 0.f;
#pragma unroll
                for (int i = 0; i < 4; ++i) yy += *LDSP(float, L_FS + 7168 + i * 128 + l15 * 8 + gq * 4);
                const unsigned wk = *CNT(C_W0 + gq);
                const int col = fb * 16 + l15, mw = col / 28, cc = col - 28 * mw;
                const int rh = cc < 24 ? (cc >> 2) & 1 : (cc - 24) >> 1, dw = cc < 24 ? (cc >> 3) * 2 + ((cc >> 1) & 1) : 6;
                const unsigned ea = L_FS + 8192 + gq * 1024 + (2 * mw + rh) * 32 + dw * 4 + (cc & 1) * 2;
                const float hn = bflo(*LDSP(uint16_t, ea)), hv = bflo(*LDSP(uint16_t, ea + 2048));
                const unsigned rb = (wk >> 16) ^ ((wk & 0x80000000u) ? 0x8000u : 0xffffu);
                const float sg = rbf(frcp(1.f + __expf(-bflo(rb))));
                const float res = hv + rbf(hn + rbf(rbf(yy) * sg));
                p.out[(size_t)(2047 - (int)(wk & 2047u)) * D + col] = __builtin_bit_cast(uint16_t, (__bf16)res);
            }
        }
        STAMP(12);
        STAMP_FLUSH();
        asm volatile(".Lfk_end_a:");
    } else {
        __builtin_amdgcn_s_setprio(2);
        const int ks0 = w3 < 3 ? 4 * w3 : 11;
        const unsigned wkv = L_WKV + (w3 < 3 ? 7168 * w3 : 20480);
        v4u xf[4];
        {
            const v4u sw = mk_srd((m & 1) ? p.wv : p.wk, D * DH * 2);
            const __amdgpu_buffer_rsrc_t xr = __builtin_amdgcn_make_buffer_rsrc((void*)(p.x + (size_t)(t0 + 16 * (m >> 1)) * D), 0, 16 * D * 2, 0x00020000);
#pragma unroll
            for (int s = 0; s < 4; ++s) xf[s] = __builtin_amdgcn_raw_buffer_load_b128(xr, l15 * (D * 2) + gq * 16 + s * 64, ks0 * 64, 0);
#pragma unroll
            for (int i = 0; i < 6; ++i) dma16(sw, lane * 16, ks0 * 1792 + i * 1024, lbase + wkv + i * 1024);
            if (w3 < 2) dma16(sw, lane * 16, ks0 * 1792 + 6144, lbase + wkv + 6144);
        }
        VMWAIT(0);
        STAMP(1);
        {
            const unsigned wa = wkv + (8 * gq + q4) * 56 + 8 * (lane & 3);
            if (w3 >= 2) xf[3] = (v4u){0u, 0u, 0u, 0u};
            v2u wf[4][4];
#pragma unroll
            for (int s = 0; s < 4; ++s) {
                const unsigned a = wa + ((s == 3 && w3 >= 2) ? 0 : s * 1792);
                wf[s][0] = trd(L + a); wf[s][1] = trd(L + a + 224); wf[s][2] = trd(L + a + 32); wf[s][3] = trd(L + a + 256);
            }
            float s1, s2;
            { float a1[2] = {0.f, 0.f}, a2[2] = {0.f, 0.f};
#pragma unroll
              for (int s = 0; s < 4; ++s)
#pragma unroll
                for (int e = 0; e < 4; ++e) { a1[e & 1] = dot2(xf[s][e], ONES2, a1[e & 1]); a2[e & 1] = dot2(xf[s][e], xf[s][e], a2[e & 1]); }
              s1 = a1[0] + a1[1]; s2 = a2[0] + a2[1]; }
            f32x4 ka[2] = {}, ca[2] = {};
            const unsigned o3 = w3 >= 2 ? 0u : ONES2;
#pragma unroll
            for (int s = 0; s < 4; ++s) {
                const unsigned ov = s == 3 ? o3 : ONES2;
                const v4u ones = {ov, ov, ov, ov};
                const bf16x8 w0 = mk8(wf[s][0], wf[s][1]), w1 = mk8(wf[s][2], wf[s][3]);
                if (m & 1) { ka[0] = MFMA16(as8(xf[s]), w0, ka[0]); ka[1] = MFMA16(as8(xf[s]), w1, ka[1]); ca[0] = MFMA16(as8(ones), w0, ca[0]); ca[1] = MFMA16(as8(ones), w1, ca[1]); }
                else { ka[0] = MFMA16(w0, as8(xf[s]), ka[0]); ka[1] = MFMA16(w1, as8(xf[s]), ka[1]); ca[0] = MFMA16(w0, as8(ones), ca[0]); ca[1] = MFMA16(w1, as8(ones), ca[1]); }
            }
            s1 = half_sum(row_sum(s1)); s2 = half_sum(row_sum(s2));
            const unsigned pb = L_KVP + w3 * 2304;
            *LDSP(f32x4, pb + lane * 16) = ka[0]; *LDSP(f32x4, pb + 1024 + lane * 16) = ka[1];
            if (m & 1) { if (lane < 16) { *LDSP(float, pb + 2048 + lane * 4) = ca[0][0]; *LDSP(float, pb + 2048 + 64 + lane * 4) = ca[1][0]; } }
            else if (l15 == 0) { *LDSP(f32x4, pb + 2048 + gq * 16) = ca[0]; *LDSP(f32x4, pb + 2048 + 64 + gq * 16) = ca[1]; }
            if (lane < 16) *LDSP(f32x2, pb + 2176 + lane * 8) = (f32x2){s1, s2};
            LGWAIT();
        }
        unsigned arr = 0;
        if (lane == 0) arr = CNT_ADD(C_KV);
        STAMP(2);
        if (__builtin_amdgcn_readfirstlane(arr) == 3u) {
            POLLMEM();
            f32x4 k0 = {}, k1 = {};
#pragma unroll
            for (int i = 0; i < 4; ++i) { k0 += *LDSP(f32x4, L_KVP + i * 2304 + lane * 16); k1 += *LDSP(f32x4, L_KVP + i * 2304 + 1024 + lane * 16); }
            v4u piece; unsigned dst;
            if (!(m & 1)) {
                f32x4 c0 = {}, c1 = {}; f32x2 ss = {};
#pragma unroll
                for (int i = 0; i < 4; ++i) { c0 += *LDSP(f32x4, L_KVP + i * 2304 + 2048 + gq * 16); c1 += *LDSP(f32x4, L_KVP + i * 2304 + 2112 + gq * 16); ss += *LDSP(f32x2, L_KVP + i * 2304 + 2176 + l15 * 8); }
                const float mean = ss.x * (1.f / D), var = fmaxf(ss.y * (1.f / D) - mean * mean, 0.f);
                const float mb = rbf(mean), rs = frcp(rbf(fsq(rbf(rbf(var) + EPS))));
                float k[8];
#pragma unroll
                for (int r = 0; r < 4; ++r) { k[r] = (k0[r] - mb * c0[r]) * rs; k[4 + r] = (k1[r] - mb * c1[r]) * rs; }
                piece = (v4u){pk(k[0], k[1]), pk(k[2], k[3]), pk(k[4], k[5]), pk(k[6], k[7])};
                if (gq == 3) { piece.z = ONES2; piece.w = 0x00003f80u; }
                dst = (gq >> 1) * 1024 + (16 * (m >> 1) + l15 + 32 * (gq & 1)) * 16;
            } else {
                float c0 = 0.f, c1 = 0.f; f32x4 sa = {}, sb = {};
#pragma unroll
                for (int i = 0; i < 4; ++i) { c0 += *LDSP(float, L_KVP + i * 2304 + 2048 + l15 * 4); c1 += *LDSP(float, L_KVP + i * 2304 + 2112 + l15 * 4);
                    sa += *LDSP(f32x4, L_KVP + i * 2304 + 2176 + gq * 32); sb += *LDSP(f32x4, L_KVP + i * 2304 + 2176 + gq * 32 + 16); }
                const float t1[4] = {sa[0], sa[2], sb[0], sb[2]}, t2[4] = {sa[1], sa[3], sb[1], sb[3]};
                float v0[4], v1[4];
#pragma unroll
                for (int r = 0; r < 4; ++r) {
                    const float mean = t1[r] * (1.f / D), var = fmaxf(t2[r] * (1.f / D) - mean * mean, 0.f);
                    const float mb = rbf(mean), rs = frcp(rbf(fsq(rbf(rbf(var) + EPS))));
                    v0[r] = (k0[r] - mb * c0) * rs; v1[r] = (k1[r] - mb * c1) * rs;
                }
                unsigned ax = pk(v0[0], v0[1]), ay = pk(v0[2], v0[3]), bx = pk(v1[0], v1[1]), by = pk(v1[2], v1[3]);
                if (l15 >= 12) { bx = by = (l15 == 12) ? ONES2 : 0u; }
                auto rx = __builtin_amdgcn_permlane16_swap(ax, bx, false, false);
                auto ry = __builtin_amdgcn_permlane16_swap(ay, by, false, false);
                piece = (v4u){rx[0], ry[0], rx[1], ry[1]};
                dst = 2048 + (m >> 1) * 1024 + lane * 16;
            }
            STAMP(10);
            BSTORE(piece, WS_KV + dst, cl * 4096);
            VMWAIT(0);
            if (lane == 0) __hip_atomic_store((gu32*)(p.ws + WS_FLAG + (cl * 4 + m) * 64), tag, RLX_AGENT);
            STAMP(13);
        }
        unsigned touch = 0;
        if (wave == 7) {
            unsigned spins = 0; unsigned ok = 0;
            do {
#pragma unroll
                for (int i = 0; i < 4; ++i) if (!(ok & (1u << i))) ok |= (__hip_atomic_load((gu32*)(p.ws + WS_FLAG + (lane + 64 * i) * 64), RLX_AGENT) == tag) ? (1u << i) : 0u;
            } while (!__all(ok == 15u) && ++spins < SPIN_CAP);
        }
        STAMP(3);
        BAR();
        __builtin_amdgcn_s_setprio(3);
        __builtin_amdgcn_s_sleep(8);
        {
            const v4u st_ = mk_srd(w3 == 0 ? p.wo : w3 == 1 ? p.wd : w3 == 2 ? p.wg : p.wu, w3 == 0 ? D * D * 2 : D * F * 2);
            const int e0 = lane, e1 = lane + 64;
            unsigned o0, o1;
            if (w3 == 0) { o0 = (jj * 98 + e0) * 128; o1 = e1 < 98 ? (jj * 98 + e1) * 128 : 0x7fffffff; }
            else if (w3 == 1) { o0 = (lane < 56 && xcd < 7) ? (jj * 56 + lane) * (D * 2) + xcd * 128 : 0x7fffffff; o1 = 0x7fffffff; }
            else { o0 = (jj * 14 + e0 / 5) * (F * 2) + ((xcd * 448) & ~127) + (e0 % 5) * 128; o1 = e1 < 70 ? (jj * 14 + e1 / 5) * (F * 2) + ((xcd * 448) & ~127) + (e1 % 5) * 128 : 0x7fffffff; }
            asm volatile("buffer_load_dword %0, %1, %2, 0 offen\n\tbuffer_load_dword %0, %3, %2, 0 offen" : "=&v"(touch) : "v"(o0), "s"(st_), "v"(o1) : "memory");
        }
        v2u xr[4], wrq[4];
        float wsum;
        {
            const v4u sx = mk_srd(p.x + (size_t)t0 * D, 32 * D * 2);
            dma16(sx, vw, (8 * w3) * D * 2, lbase + L_OI + (2 * w3) * 1024); dma16(sx, vw, (8 * w3 + 4) * D * 2, lbase + L_OI + (2 * w3 + 1) * 1024);
            float s = 0.f;
            v4u v = {0u, 0u, 0u, 0u};
            if (lane < 56) v = *(const GAS v4u*)(p.wr + lane * 8);
#pragma unroll
            for (int i = 0; i < 4; ++i) wrq[i] = *(const GAS v2u*)(p.wr + (28 * m + 7 * w3 + min(2 * i + hh, 6)) * 4);
#pragma unroll
            for (int e = 0; e < 4; ++e) s = dot2(v[e], ONES2, s);
            s += dppf<XOR1>(s); s += dppf<XOR2>(s); s += dppf<XOR7>(s); s += dppf<MIR15>(s);
            wsum = half_sum(row_sum(s));
        }
        bf16x8 WO[32];
        {
            const v4u so = mk_srd(p.wo, D * D * 2);
            const unsigned b = 56 * w3 + 32 * (gq & 1) + 8 * (lane & 3), r3 = 2 * hh + (q4 & 1);
            const unsigned a0 = L_ST + (4 * hh + q4) * 256 + ((((b >> 4) + 4 * q4) & 15) << 4) + (b & 8);
            const unsigned a3 = L_ST + (24 + r3) * 256 + ((((b >> 4) + 4 * r3) & 15) << 4) + (b & 8);
#pragma unroll
            for (int bt = 0; bt < 4; ++bt) {
#pragma unroll
                for (int s = 0; s < 7; ++s) dma16(so, vw, (112 * bt + 28 * w3 + 4 * s) * D * 2, lbase + L_ST + w3 * 7168 + s * 1024);
                VMWAIT(0);
                if (lane == 0) CNT_ADD(C_WO);
                { unsigned spins = 0; while (CNT_LD(C_WO) < 8u * bt + 4u && ++spins < SPIN_CAP) __builtin_amdgcn_s_sleep(1); POLLMEM(); }
                if (bt == 0) {
                    asm volatile("" : "+v"(touch));
#pragma unroll
                    for (int i = 0; i < 4; ++i) { const unsigned bb = 56 * w3 + 8 * min(2 * i + hh, 6);
                        xr[i] = *LDSP(v2u, L_OI + l31 * 256 + ((((bb >> 4) + 4 * (l31 & 3)) & 15) << 4) + (bb & 8)); }
                }
#pragma unroll
                for (int h = 0; h < 4; ++h) {
                    WO[8 * bt + 2 * h] = mk8(trd(L + a0 + h * 7168), trd(L + a0 + h * 7168 + 2048));
                    WO[8 * bt + 2 * h + 1] = mk8(trd(L + a0 + h * 7168 + 4096), trd(L + a3 + h * 7168));
                }
                LGWAIT();
#pragma unroll
                for (int s = 0; s < 8; ++s) asm volatile("" : "+v"(WO[8 * bt + s]));
                if (bt == 0) asm volatile("" : "+v"(xr[0]), "+v"(xr[1]), "+v"(xr[2]), "+v"(xr[3]));
                if (bt < 3) {
                    if (lane == 0) CNT_ADD(C_WO);
                    { unsigned spins = 0; while (CNT_LD(C_WO) < 8u * bt + 8u && ++spins < SPIN_CAP) __builtin_amdgcn_s_sleep(1); POLLMEM(); }
                }
            }
            if (isA) {
                const v4u sg = mk_srd((w3 & 1) ? p.wu : p.wg, D * F * 2);
                const unsigned vo = lane * (F * 2) + (xcd * 28 + jj) * 16;
#pragma unroll
                for (int s = 0; s < 4; ++s) if (s < 3 || w3 < 2) { dma16(sg, vo, (4 * (w3 >> 1) + s) * 64 * F * 2, lbase + L_FA + (w3 & 1) * 7168 + (4 * (w3 >> 1) + s) * 1024); VMWAIT(0); }
            }
            unsigned t2 = __builtin_amdgcn_raw_buffer_load_b32(wsr, WS_O + lane * 128, (cl * 4 + w3) * 8192, 16);
            if (w3 == 0) t2 ^= __builtin_amdgcn_raw_buffer_load_b32(wsr, WS_CAND + lane * 128, 0, 16);
            if (w3 == 1) t2 ^= __builtin_amdgcn_raw_buffer_load_b32(wsr, (lane < 16 ? WS_STAT + cl * 2048 : WS_ROW + cl * 4096 - 2048) + lane * 128, 0, 16);
            if (w3 == 2) t2 ^= *(const GAS unsigned*)(p.out + (size_t)(t0 + l31) * D + 112 * m);
            asm volatile("" :: "v"(t2));
            VMWAIT(0);
        }
        STAMP(4);
        BAR();
        BAR();
        STAMP(5);
        const unsigned salt = (tag * 0x9E3779B1u) | 1u;
        f32x16 acc = {};
#define OPROJ(mm) do { v4u of_[8]; _Pragma("unroll") for (int s_ = 0; s_ < 8; ++s_) of_[s_] = *LDSP(v4u, L_OI + (mm) * 8192 + s_ * 1024 + lane * 16); \
            _Pragma("unroll") for (int s_ = 0; s_ < 8; ++s_) acc = MFMA32(WO[8 * (mm) + s_], as8(of_[s_]), acc); } while (0)
        if (m == 0) OPROJ(0); else if (m == 1) OPROJ(1); else if (m == 2) OPROJ(2); else OPROJ(3);
        BAR();
        STAMP(6);
        if (m != 0) OPROJ(0);
        if (m != 1) OPROJ(1);
        if (m != 2) OPROJ(2);
        if (m != 3) OPROJ(3);
#undef OPROJ
        float hv[16];
        {
            float s1 = 0.f, s2 = 0.f, s3 = 0.f;
#pragma unroll
            for (int i = 0; i < 4; ++i) {
                const float xv[4] = {bflo(xr[i].x), bfhi(xr[i].x), bflo(xr[i].y), bfhi(xr[i].y)};
                const float wv[4] = {bflo(wrq[i].x), bfhi(wrq[i].x), bflo(wrq[i].y), bfhi(wrq[i].y)};
#pragma unroll
                for (int r = 0; r < 4; ++r) {
                    float h = rbf(xv[r] + rbf(acc[4 * i + r]));
                    if (i == 3 && hh) h = 0.f;
                    hv[4 * i + r] = h; s1 += h; s2 += h * h; s3 += h * wv[r];
                }
            }
            s1 = half_sum(s1); s2 = half_sum(s2); s3 = half_sum(s3);
            if (hh == 0) *LDSP(f32x4, L_KVP + w3 * 512 + l31 * 16) = (f32x4){s1, s2, s3, 0.f};
            LGWAIT();
            unsigned ar2 = 0;
            if (lane == 0) ar2 = CNT_ADD(C_ST);
            if (__builtin_amdgcn_readfirstlane(ar2) == 3u) {
                POLLMEM();
                f32x4 a = *LDSP(f32x4, L_KVP + l31 * 16);
#pragma unroll
                for (int i = 1; i < 4; ++i) a += *LDSP(f32x4, L_KVP + i * 512 + l31 * 16);
                v4u v = __builtin_bit_cast(v4u, a); v.w = x3(v.x, v.y, v.z) ^ salt;
                if (hh == 0) BSTORE(v, WS_STAT + l31 * 16, (cl * 4 + m) * 512);
            }
        }
        STAMP(7);
        float mean_b, rsd; unsigned key;
        {
            v4u r0, r1; unsigned spins = 0;
            do { POLLMEM(); r0 = BLOAD(WS_STAT + lane * 16, cl * 2048); r1 = BLOAD(WS_STAT + 1024 + lane * 16, cl * 2048); }
            while (!__all(x4(r0) == salt && x4(r1) == salt) && ++spins < SPIN_CAP);
            STAMP(8);
            const float s1 = half_sum(__uint_as_float(r0.x) + __uint_as_float(r1.x)), s2 = half_sum(__uint_as_float(r0.y) + __uint_as_float(r1.y)), s3 = half_sum(__uint_as_float(r0.z) + __uint_as_float(r1.z));
            const float mean = s1 * (1.f / D), var = fmaxf(s2 * (1.f / D) - mean * mean, 0.f);
            mean_b = rbf(mean); rsd = frcp(rbf(fsq(rbf(rbf(var) + EPS))));
            const float rr = (s3 - mean_b * wsum) * rsd;
            const unsigned rb = __builtin_bit_cast(uint16_t, (__bf16)rr);
            key = ((rb ^ ((rb & 0x8000u) ? 0xffffu : 0x8000u)) << 16) | (unsigned)(2047 - (t0 + l31));
        }
        const unsigned k0 = umax32(key), k1 = umax32(key == k0 ? 0u : key);
        const bool cand = key == k0 || key == k1;
        if (m == 0 && wave == 4 && lane == 0) BSTORE(((v4u){k0, tag, k1, tag}), WS_CAND, cl * 128);
        unsigned nd[8], hd[8], od[8];
#pragma unroll
        for (int i = 0; i < 8; ++i) {
            const float n0 = rbf(rbf(hv[2 * i] - mean_b) * rsd), n1 = rbf(rbf(hv[2 * i + 1] - mean_b) * rsd);
            nd[i] = pk(n0, n1); hd[i] = pk(hv[2 * i], hv[2 * i + 1]); od[i] = pk(hv[2 * i] + n0, hv[2 * i + 1] + n1);
        }
        {
            auto sn = __builtin_amdgcn_permlane32_swap(nd[6], nd[7], false, false);
            auto sh = __builtin_amdgcn_permlane32_swap(hd[6], hd[7], false, false);
            v4u na = {nd[0], nd[1], nd[2], nd[3]}, nb = {nd[4], nd[5], sn[0], 0u}, ha = {hd[0], hd[1], hd[2], hd[3]}, hb = {hd[4], hd[5], sh[0], 0u};
            nb.w = x8(na, nb) ^ salt; hb.w = x8(ha, hb) ^ salt;
            if (cand) {
                const unsigned ro = WS_ROW + (cl * 2 + (key == k1)) * 2048 + ((m * 4 + w3) * 2 + hh) * 32;
                BSTORE(na, ro, 0); BSTORE(nb, ro + 16, 0); BSTORE(ha, ro + 1024, 0); BSTORE(hb, ro + 1040, 0);
            }
        }
        if (lane == 0) CNT_ADD(C_CO);
        STAMP(9);
        GAS v2u* orow = (GAS v2u*)(p.out + (size_t)(t0 + l31) * D + 112 * m + 28 * w3 + 4 * hh);
        if (!cand) {
#pragma unroll
            for (int i = 0; i < 4; ++i) if (i < 3 || hh == 0) orow[2 * i] = (v2u){od[2 * i], od[2 * i + 1]};
        }
        { unsigned spins = 0; while (CNT_LD(C_WINOK) == 0u && ++spins < SPIN_CAP * 4) __builtin_amdgcn_s_sleep(4); POLLMEM(); }
        if (cand && key != *CNT(C_W0) && key != *CNT(C_W1)) {
#pragma unroll
            for (int i = 0; i < 4; ++i) if (i < 3 || hh == 0) orow[2 * i] = (v2u){od[2 * i], od[2 * i + 1]};
        }
        STAMP(12);
        STAMP_FLUSH();
        asm volatile(".Lfk_end_b:");
    }
}
}

extern "C" void kernel_launch(void* const* d_in, const int* in_sizes, int n_in, void* d_out, int out_size, void* d_ws, size_t ws_size, hipStream_t stream) {
    Params p;
    p.x = (const uint16_t*)d_in[0]; p.wq = (const uint16_t*)d_in[1]; p.wk = (const uint16_t*)d_in[2]; p.wv = (const uint16_t*)d_in[3]; p.wo = (const uint16_t*)d_in[4];
    p.wr = (const uint16_t*)d_in[6]; p.wg = (const uint16_t*)d_in[7]; p.wu = (const uint16_t*)d_in[8]; p.wd = (const uint16_t*)d_in[9];
    p.out = (uint16_t*)d_out; p.ws = (unsigned char*)d_ws;
    fused_kernel<<<NWG, NTHR, 0, stream>>>(p);
}
```

```cpp
#include <hip/hip_runtime.h>
#include <stdint.h>

#define GAS __attribute__((address_space(1)))
#define LAS __attribute__((address_space(3)))
#define CAS __attribute__((address_space(4)))
typedef unsigned v4u __attribute__((ext_vector_type(4)));
typedef unsigned v2u __attribute__((ext_vector_type(2)));
typedef float f32x16 __attribute__((ext_vector_type(16)));
typedef float f32x8 __attribute__((ext_vector_type(8)));
typedef float f32x4 __attribute__((ext_vector_type(4)));
typedef float f32x2 __attribute__((ext_vector_type(2)));
typedef __bf16 bf16x2 __attribute__((ext_vector_type(2)));
typedef __bf16 bf16x8 __attribute__((ext_vector_type(8)));
typedef __bf16 bf16x16 __attribute__((ext_vector_type(16)));
typedef short v4i16 __attribute__((ext_vector_type(4)));
typedef GAS unsigned gu32;
typedef GAS unsigned long long gu64;
#define RLX_AGENT __ATOMIC_RELAXED, __HIP_MEMORY_SCOPE_AGENT

namespace {
constexpr int S = 2048, D = 448, DH = 28, F = 1792;
constexpr int NWG = 256, NTHR = 512;
constexpr float QSCALE = 0.18898223650461361f * 1.4426950408889634f;
constexpr float EPS = 1e-5f;
constexpr unsigned SPIN_CAP = 1u << 18;

constexpr unsigned WS_EPOCH = 0;
constexpr unsigned WS_CAND = 4096;
constexpr unsigned WS_ACT = WS_CAND + 8192;
constexpr unsigned WS_KV = 65536;
constexpr unsigned WS_CK = WS_KV + 262144;
constexpr unsigned WS_STAT = WS_CK + 262144;
constexpr unsigned WS_ROW = WS_STAT + 131072;
constexpr unsigned WS_O = WS_ROW + 128 * 2048;
constexpr unsigned WS_END = WS_O + 256 * 8192;
constexpr unsigned WS_DBG = 8u << 20;

constexpr unsigned L_XI = 0;
constexpr unsigned L_RING = 28672;
constexpr unsigned RINGB = 24576, R_QI = 22528;
constexpr unsigned L_WKV = L_RING + 4 * RINGB;
constexpr unsigned L_KVP = L_WKV + 26624;
constexpr unsigned L_STAT = L_KVP + 4 * 2304;
constexpr unsigned L_CSQ = L_STAT + 256;
constexpr unsigned L_CNT = L_CSQ + 512;
constexpr unsigned L_FLG = L_CNT + 64;
constexpr unsigned LDS_BYTES = L_FLG + 96;
constexpr unsigned L_ST = 0, L_RED = 28672, L_OI = L_RED + 49152, L_FA = L_WKV, L_FS = 57344;
static_assert(LDS_BYTES <= 163840 && L_OI + 32768 <= L_WKV, "lds");
enum { C_X = 0, C_KV = 1, C_ST = 2, C_FB = 3, C_WINOK = 4, C_WO = 5, C_W0 = 6, C_W1 = 7, C_CO = 8, C_DM = 9, C_T13 = 10 };

struct Params {
    const uint16_t* x; const uint16_t* wq; const uint16_t* wk; const uint16_t* wv; const uint16_t* wo;
    const uint16_t* wr; const uint16_t* wg; const uint16_t* wu; const uint16_t* wd;
    uint16_t* out; unsigned char* ws;
};

#define SBAR() __builtin_amdgcn_sched_barrier(0)
#define BAR() asm volatile("s_waitcnt lgkmcnt(0)\n\ts_barrier" ::: "memory")
#define VMWAIT(n) asm volatile("s_waitcnt vmcnt(" #n ")" ::: "memory")
#define LGWAIT() asm volatile("s_waitcnt lgkmcnt(0)" ::: "memory")

__device__ __forceinline__ v4u mk_srd(const void* p, unsigned bytes) {
    const unsigned long long a = (unsigned long long)p;
    v4u r; r.x = (unsigned)a; r.y = (unsigned)(a >> 32) & 0xffffu; r.z = bytes; r.w = 0x00020000u; return r;
}
__device__ __forceinline__ void dma16(v4u srd, unsigned voff, unsigned soff, unsigned ldsaddr) {
    asm volatile("s_mov_b32 m0, %0\n\ts_nop 0\n\tbuffer_load_dwordx4 %1, %2, %3 offen lds" :: "s"(ldsaddr), "v"(voff), "s"(srd), "s"(soff) : "memory");
}
template <int IMM> __device__ __forceinline__ v4u aload16(v4u srd, unsigned voff, unsigned soff) {
    v4u r; asm volatile("buffer_load_dwordx4 %0, %1, %2, %3 offen offset:%4" : "=v"(r) : "v"(voff), "s"(srd), "s"(soff), "i"(IMM) : "memory"); return r;
}
__device__ __forceinline__ unsigned pk(float lo, float hi) {
    f32x2 v = {lo, hi}; return __builtin_bit_cast(unsigned, __builtin_convertvector(v, bf16x2));
}
__device__ __forceinline__ float bflo(unsigned u) { return __uint_as_float(u << 16); }
__device__ __forceinline__ float bfhi(unsigned u) { return __uint_as_float(u & 0xffff0000u); }
__device__ __forceinline__ float rbf(float f) { return (float)(__bf16)f; }
__device__ __forceinline__ v2u trd(LAS char* p) { return __builtin_bit_cast(v2u, __builtin_amdgcn_ds_read_tr16_b64_v4i16((LAS v4i16*)p)); }
__device__ __forceinline__ bf16x8 mk8(v2u a, v2u b) { v4u r = {a.x, a.y, b.x, b.y}; return __builtin_bit_cast(bf16x8, r); }
__device__ __forceinline__ bf16x8 as8(v4u a) { return __builtin_bit_cast(bf16x8, a); }
__device__ __forceinline__ float dot2(unsigned a, unsigned b, float c) { return __builtin_amdgcn_fdot2_f32_bf16(__builtin_bit_cast(bf16x2, a), __builtin_bit_cast(bf16x2, b), c, false); }
template <int CTRL> __device__ __forceinline__ float dppf(float x) { return __builtin_bit_cast(float, __builtin_amdgcn_mov_dpp(__builtin_bit_cast(int, x), CTRL, 0xf, 0xf, true)); }
template <int CTRL> __device__ __forceinline__ unsigned dppu(unsigned x) { return (unsigned)__builtin_amdgcn_mov_dpp((int)x, CTRL, 0xf, 0xf, true); }
constexpr int XOR1 = 0xB1, XOR2 = 0x4E, XOR7 = 0x141, ROR8 = 0x128, MIR15 = 0x140;
__device__ __forceinline__ float half_sum(float v) { auto r = __builtin_amdgcn_permlane32_swap(__float_as_uint(v), __float_as_uint(v), false, false); return __uint_as_float(r[0]) + __uint_as_float(r[1]); }
__device__ __forceinline__ unsigned umax32(unsigned v) {
    v = max(v, dppu<XOR1>(v)); v = max(v, dppu<XOR2>(v)); v = max(v, dppu<XOR7>(v)); v = max(v, dppu<MIR15>(v));
    auto r = __builtin_amdgcn_permlane16_swap(v, v, false, false); return max(r[0], r[1]);
}
__device__ __forceinline__ unsigned umax64(unsigned v) { v = umax32(v); auto r = __builtin_amdgcn_permlane32_swap(v, v, false, false); return max(r[0], r[1]); }
#define MFMA32(a, b, c) __builtin_amdgcn_mfma_f32_32x32x16_bf16(a, b, c, 0, 0, 0)
#define MFMA16(a, b, c) __builtin_amdgcn_mfma_f32_16x16x32_bf16(a, b, c, 0, 0, 0)
__device__ __forceinline__ f32x16 smf_lo(bf16x8 v, const f32x16& p, f32x16 o, int idx) {
    f32x8 h = __builtin_shufflevector(p, p, 0, 1, 2, 3, 4, 5, 6, 7);
    return __builtin_amdgcn_smfmac_f32_32x32x32_bf16(v, __builtin_bit_cast(bf16x16, h), o, idx, 0, 0);
}
__device__ __forceinline__ f32x16 smf_hi(bf16x8 v, const f32x16& p, f32x16 o, int idx) {
    f32x8 h = __builtin_shufflevector(p, p, 8, 9, 10, 11, 12, 13, 14, 15);
    return __builtin_amdgcn_smfmac_f32_32x32x32_bf16(v, __builtin_bit_cast(bf16x16, h), o, idx, 0, 0);
}
#define LDSP(T, off) ((LAS T*)(L + (off)))
#define BLOAD(off, soff) __builtin_amdgcn_raw_buffer_load_b128(wsr, (off), (soff), 16)
#define BSTORE(v, off, soff) __builtin_amdgcn_raw_buffer_store_b128((v), wsr, (off), (soff), 16)

#define STAMP(i) do {} while (0)
#define STAMPC(i) do {} while (0)
#define STAMP_DECL do {} while (0)
#define STAMP_FLUSH() do {} while (0)
#define POLLMEM() asm volatile("" ::: "memory")

__device__ __forceinline__ unsigned x3(unsigned a, unsigned b, unsigned c) { return __builtin_amdgcn_bitop3_b32(a, b, c, 0x96); }
__device__ __forceinline__ unsigned x4(v4u a) { return x3(a.x, a.y, a.z) ^ a.w; }
__device__ __forceinline__ unsigned x8(v4u a, v4u b) { return x3(x3(a.x, a.y, a.z), x3(a.w, b.x, b.y), x3(b.z, b.w, 0u)); }
__device__ __forceinline__ float frcp(float x) { return __builtin_amdgcn_rcpf(x); }
__device__ __forceinline__ float fsq(float x) { return __builtin_amdgcn_sqrtf(x); }
__device__ __forceinline__ float row_sum(float v) { auto r = __builtin_amdgcn_permlane16_swap(__float_as_uint(v), __float_as_uint(v), false, false); return __uint_as_float(r[0]) + __uint_as_float(r[1]); }
template <int KIND> __device__ __forceinline__ void stouch(const void* base, int n) {
    unsigned long long a = (unsigned long long)base;
    for (int i = 0; i < n; ++i) {
        unsigned d;
        if (KIND == 0) {
            asm volatile("s_load_dword %0, %1, 0\n\t""s_load_dword %0, %1, 64\n\t""s_load_dword %0, %1, 128\n\t""s_load_dword %0, %1, 192\n\t""s_load_dword %0, %1, 256\n\t""s_load_dword %0, %1, 320\n\t""s_load_dword %0, %1, 384\n\t""s_load_dword %0, %1, 448\n\t""s_load_dword %0, %1, 512\n\t""s_load_dword %0, %1, 576\n\t""s_load_dword %0, %1, 640\n\t""s_load_dword %0, %1, 704\n\t""s_load_dword %0, %1, 768\n\t""s_load_dword %0, %1, 832\n\t""s_waitcnt lgkmcnt(0)" : "=&s"(d) : "s"(a) : "memory"); a += 896ull;
        } else if (KIND == 1) {
            asm volatile("s_load_dword %0, %1, 0\n\t""s_load_dword %0, %1, 64\n\t""s_load_dword %0, %1, 128\n\t""s_load_dword %0, %1, 192\n\t""s_load_dword %0, %1, 256\n\t""s_load_dword %0, %1, 320\n\t""s_load_dword %0, %1, 384\n\t""s_load_dword %0, %1, 3584\n\t""s_load_dword %0, %1, 3648\n\t""s_load_dword %0, %1, 3712\n\t""s_load_dword %0, %1, 3776\n\t""s_load_dword %0, %1, 3840\n\t""s_load_dword %0, %1, 3904\n\t""s_load_dword %0, %1, 3968\n\t""s_waitcnt lgkmcnt(0)" : "=&s"(d) : "s"(a) : "memory"); a += 7168ull;
        } else {
            asm volatile("s_load_dword %0, %1, 0\n\t""s_load_dword %0, %1, 64\n\t""s_load_dword %0, %1, 896\n\t""s_load_dword %0, %1, 960\n\t""s_load_dword %0, %1, 1792\n\t""s_load_dword %0, %1, 1856\n\t""s_load_dword %0, %1, 2688\n\t""s_load_dword %0, %1, 2752\n\t""s_load_dword %0, %1, 3584\n\t""s_load_dword %0, %1, 3648\n\t""s_load_dword %0, %1, 4480\n\t""s_load_dword %0, %1, 4544\n\t""s_load_dword %0, %1, 5376\n\t""s_load_dword %0, %1, 5440\n\t""s_waitcnt lgkmcnt(0)" : "=&s"(d) : "s"(a) : "memory"); a += 6272ull;
        }
    }
}
#define CNT(i) ((LAS unsigned*)(L + L_CNT) + (i))
#define CNT_ADD(i) __hip_atomic_fetch_add(CNT(i), 1u, __ATOMIC_RELAXED, __HIP_MEMORY_SCOPE_WORKGROUP)
#define CNT_LD(i) __hip_atomic_load(CNT(i), __ATOMIC_RELAXED, __HIP_MEMORY_SCOPE_WORKGROUP)
#define ONES2 0x3f803f80u
enum { F_X = 0, F_DM = 1, F_T13 = 2, F_CO = 3, F_WO = 4 };
#define FLG_SET(f, v) do { if (lane == 0) __hip_atomic_store((LAS unsigned*)(L + L_FLG + (f) * 16 + w3 * 4), (unsigned)(v), __ATOMIC_RELAXED, __HIP_MEMORY_SCOPE_WORKGROUP); } while (0)
#define FLG_WAIT(f, v, slp) do { unsigned sp_ = 0; for (;;) { POLLMEM(); const v4u f_ = *(volatile LAS v4u*)(L + L_FLG + (f) * 16); \
        if (min(min(f_.x, f_.y), min(f_.z, f_.w)) >= (unsigned)(v) || ++sp_ >= SPIN_CAP) break; if (slp) __builtin_amdgcn_s_sleep(slp); } POLLMEM(); } while (0)

extern "C" __global__ __launch_bounds__(NTHR, 2) void fused_kernel(Params p) {
    __shared__ __attribute__((aligned(16))) char lds_[LDS_BYTES];
    LAS char* L = (LAS char*)lds_;
    const unsigned lbase = (unsigned)(uintptr_t)L;
    const int g = blockIdx.x, xcd = g & 7, jj = g >> 3, cl = xcd * 8 + (jj >> 2), m = jj & 3, t0 = cl * 32;
    const int tid = threadIdx.x, lane = tid & 63, wave = __builtin_amdgcn_readfirstlane(tid >> 6);
    const int l31 = lane & 31, hh = lane >> 5, l15 = lane & 15, gq = lane >> 4, q4 = (lane & 15) >> 2;
    const int w3 = wave & 3;
    STAMP_DECL;
    STAMP(0);
    unsigned ctouch = 0;
    if (wave == 3) {
        unsigned long long base; unsigned len, len2;
        asm volatile("s_getpc_b64 %0\n.Lfk_here:\n\ts_mov_b32 %1, .Lfk_end_a-.Lfk_here\n\ts_mov_b32 %2, .Lfk_end_b-.Lfk_here" : "=s"(base), "=s"(len), "=s"(len2));
        len = max(len, len2);
        unsigned off = (unsigned)((jj & 3) * 64 + lane) * 128u;
        off = off < len - 4u ? off : 0u;
        asm volatile("global_load_dword %0, %1, %2" : "=v"(ctouch) : "v"(off), "s"(base) : "memory");
    }
    if (tid < 40) *CNT(tid) = 0u;
    BAR();
    const unsigned tag = *(const CAS unsigned*)(p.ws + WS_EPOCH) + 1u;
    const __amdgpu_buffer_rsrc_t wsr = __builtin_amdgcn_make_buffer_rsrc(p.ws, 0, WS_END, 0x00020000);
    const unsigned vchunk = (l15 - 4 * gq) & 15;
    const unsigned vw = gq * (D * 2) + 224 * m + (vchunk < 14 ? vchunk : 13) * 16;
    const unsigned sk0 = (tag * 0x85EBCA6Bu) | 1u, sk1 = (tag * 0xC2B2AE35u) & ~1u, sv0 = (tag * 0x27D4EB2Fu) & ~1u, sv1 = (tag * 0x165667B1u) & ~1u;
    const bool isA = jj < 28;
    const int fb = xcd * 4 + (jj - 28);
    const bool isB = !isA && fb < 28;

    if (wave < 4) {
        const unsigned ring = L_RING + w3 * RINGB;
        const v4u sq = mk_srd(p.wq, D * D * 2);
        unsigned tq, tb = 0;
        {
            const v4u sx = mk_srd(p.x + (size_t)t0 * D, 32 * D * 2);
            const int row = 8 * w3 + (lane >> 3);
            const unsigned vx = row * (D * 2) + (((lane & 7) ^ ((row >> 1) & 7)) << 4);
#pragma unroll
            for (int cb = 0; cb < 7; ++cb) dma16(sx, vx, cb * 128, lbase + L_XI + (w3 * 7 + cb) * 1024);
        }
        {
            const unsigned ln = jj * 98 + 25 * w3 + lane;
            asm volatile("buffer_load_dword %0, %1, %2, 0 offen" : "=v"(tq) : "v"(lane < 25 ? ln * 128 : 0x7fffffffu), "s"(sq) : "memory");
            {
                const void* a = p.wo;
                a = l15 == 1 && wave == 2 ? (const void*)p.wr : a; a = l15 == 2 && wave == 2 ? (const void*)p.wg : a; a = l15 == 3 && wave == 2 ? (const void*)p.wu : a; a = l15 == 4 && wave == 2 ? (const void*)p.wd : a;
                a = l15 == 5 && wave == 2 ? (const void*)p.out : a; a = l15 == 6 && wave == 2 ? (const void*)(p.ws + WS_KV) : a; a = l15 == 7 && wave == 2 ? (const void*)(p.ws + WS_END - 64) : a;
                asm volatile("global_load_dword %0, %1, off" : "=v"(tb) : "v"(a) : "memory");
            }
        }
        const unsigned vq = (lane >> 2) * (D * 2) + 224 * m + ((((w3 * 7) >> 1) + (lane & 3)) << 4);
#define QDMA(gr) do { _Pragma("unroll") for (int s_ = 4 * (gr); s_ < 4 * (gr) + 4; ++s_) dma16(sq, vq, s_ * 16 * D * 2, lbase + ring + (s_ % 24) * 1024); } while (0)
        VMWAIT(2);
        asm volatile("" : "+v"(ctouch));
        STAMP(1);
        {
            v4u v[7];
#pragma unroll
            for (int cb = 0; cb < 7; ++cb) v[cb] = *LDSP(v4u, L_XI + (w3 * 7 + cb) * 1024 + lane * 16);
            QDMA(0); QDMA(1); QDMA(2);
            float s1[4] = {0.f, 0.f, 0.f, 0.f}, s2[4] = {0.f, 0.f, 0.f, 0.f};
#pragma unroll
            for (int cb = 0; cb < 7; ++cb)
#pragma unroll
                for (int e = 0; e < 4; ++e) { s1[e] = dot2(v[cb][e], ONES2, s1[e]); s2[e] = dot2(v[cb][e], v[cb][e], s2[e]); }
            float a = (s1[0] + s1[1]) + (s1[2] + s1[3]), b = (s2[0] + s2[1]) + (s2[2] + s2[3]);
            a += dppf<XOR1>(a); b += dppf<XOR1>(b); a += dppf<XOR2>(a); b += dppf<XOR2>(b); a += dppf<XOR7>(a); b += dppf<XOR7>(b);
            const float mean = a * (1.f / D), var = fmaxf(b * (1.f / D) - mean * mean, 0.f), mb = rbf(mean);
#pragma unroll
            for (int cb = 0; cb < 7; ++cb) {
#pragma unroll
                for (int e = 0; e < 4; ++e) v[cb][e] = pk(bflo(v[cb][e]) - mb, bfhi(v[cb][e]) - mb);
                *LDSP(v4u, L_XI + (w3 * 7 + cb) * 1024 + lane * 16) = v[cb];
            }
            if ((lane & 7) == 0) *LDSP(float, L_STAT + (8 * w3 + (lane >> 3)) * 4) = frcp(rbf(fsq(rbf(rbf(var) + EPS)))) * QSCALE;
            LGWAIT();
            FLG_SET(F_X, 1);
        }
        QDMA(3); QDMA(4); QDMA(5);
        FLG_WAIT(F_X, 1, 0);
        STAMP(2);
        v4u XF[28];
        {
            unsigned xa[4];
#pragma unroll
            for (int i = 0; i < 4; ++i) xa[i] = L_XI + (l31 >> 3) * 7168 + (l31 & 7) * 128 + (((2 * i + hh) ^ ((l31 >> 1) & 7)) << 4);
#pragma unroll
            for (int s = 0; s < 28; ++s) XF[s] = *LDSP(v4u, xa[s & 3] + (s >> 2) * 1024);
        }
        f32x16 acc = {};
        const float qsc = *LDSP(float, L_STAT + l31 * 4);
        const unsigned trq = ring + (8 * hh + q4) * 64 + 8 * (w3 & 1) + 32 * (gq & 1) + 8 * (lane & 3);
        v2u WA[2][8];
#define QRD(gr) do { _Pragma("unroll") for (int i_ = 0; i_ < 4; ++i_) { WA[(gr) & 1][2 * i_] = trd(L + trq + ((4 * (gr) + i_) % 24) * 1024); WA[(gr) & 1][2 * i_ + 1] = trd(L + trq + ((4 * (gr) + i_) % 24) * 1024 + 256); } } while (0)
#define QMM(gr) do { _Pragma("unroll") for (int i_ = 0; i_ < 4; ++i_) acc = MFMA32(mk8(WA[(gr) & 1][2 * i_], WA[(gr) & 1][2 * i_ + 1]), as8(XF[4 * (gr) + i_]), acc); SBAR(); } while (0)
        const int rot = (g >> 2) & 15;
        v4u KB[4][2], VB[4][2], CK[4];
        const unsigned kvo = lane * 16, cko = lane * 16 + 262144;
        const v4u skf = mk_srd(p.ws + WS_KV, 524288);
        const v4u skv = skf;
        const unsigned sall = sk0 ^ sk1 ^ sv0 ^ sv1;
        unsigned so = (unsigned)(((4 * rot + w3) & 63) << 12);
#define KVLD_(dst, vo, imm, sof, srd) asm volatile("buffer_load_dwordx4 %0, %1, %2, %3 offen offset:" #imm " sc1" : "=v"(dst) : "v"(vo), "s"(srd), "s"(sof) : "memory")
#define KVLD(dst, vo, imm, sof) KVLD_(dst, vo, imm, sof, skv)
#define LDT(b, sof) do { KVLD_(KB[b][0], kvo, 0, sof, skf); KVLD_(KB[b][1], kvo, 1024, sof, skf); KVLD_(VB[b][0], kvo, 2048, sof, skf); KVLD_(VB[b][1], kvo, 3072, sof, skf); KVLD_(CK[b], cko, 0, sof, skf); } while (0)
#define ADV() so = (so + 16384u) & 0x3ffffu
#define PINT(n, b) asm volatile("s_waitcnt vmcnt(" #n ")" : "+v"(KB[b][0]), "+v"(KB[b][1]), "+v"(VB[b][0]), "+v"(VB[b][1]), "+v"(CK[b]) :: "memory")
#define CHK(b) (x3(x3(x8(KB[b][0], KB[b][1]), x8(VB[b][0], VB[b][1]), sall), x3(CK[b].x, CK[b].y, CK[b].z), CK[b].w))
#define FIXT(b, back) do { unsigned sp_ = 0; const unsigned sb_ = (so - (back) * 16384u) & 0x3ffffu; \
            do { LDT(b, sb_); PINT(0, b); } while (__any(CHK(b) != 0u) && ++sp_ < SPIN_CAP); } while (0)
        VMWAIT(20); asm volatile("" : "+v"(tq), "+v"(tb)); QRD(0);
        VMWAIT(16); QRD(1); SBAR(); QMM(0);
        QDMA(6);
        VMWAIT(16); QRD(2); SBAR(); QMM(1);
        VMWAIT(12); QRD(3); SBAR(); QMM(2);
        VMWAIT(8); QRD(4); SBAR(); QMM(3);
        VMWAIT(4); QRD(5); SBAR(); QMM(4);
        VMWAIT(0); QRD(6); SBAR();
        LDT(0, so); ADV(); LDT(1, so); ADV(); LDT(2, so); ADV(); SBAR();
        QMM(5);
        QMM(6);
#undef QRD
#undef QMM
#undef QDMA
        {
            unsigned qp[8];
#pragma unroll
            for (int i = 0; i < 8; ++i) qp[i] = pk(acc[2 * i] * qsc, acc[2 * i + 1] * qsc);
            if (hh) { qp[6] = 0u; qp[7] = 0u; }
            *LDSP(v4u, ring + R_QI + lane * 16) = (v4u){qp[0], qp[1], qp[4], qp[5]};
            *LDSP(v4u, ring + R_QI + 1024 + lane * 16) = (v4u){qp[2], qp[3], qp[6], qp[7]};
        }
        STAMP(3);
        LGWAIT();
        FLG_SET(F_DM, 1);
        FLG_WAIT(F_DM, 1, 0);
        STAMP(4);
        f32x16 O[4] = {}, Sb[4];
        const int idx = (int)0xDDDDDDDDu;
        bf16x8 Q[4][2];
#pragma unroll
        for (int i = 0; i < 4; ++i) {
            const unsigned qa = L_RING + ((w3 + 1 + i) & 3) * RINGB + R_QI + lane * 16;
            Q[i][0] = as8(*LDSP(v4u, qa)); Q[i][1] = as8(*LDSP(v4u, qa + 1024));
        }
        asm volatile("" : "+v"(Q[0][0]), "+v"(Q[0][1]), "+v"(Q[1][0]), "+v"(Q[1][1]), "+v"(Q[2][0]), "+v"(Q[2][1]), "+v"(Q[3][0]), "+v"(Q[3][1]));
        VB[3][1] = (v4u){0u, 0u, 0u, 0u};
        Sb[3] = (f32x16){};
        PINT(10, 0);
        if (__any(CHK(0) != 0u)) FIXT(0, 3);
        Sb[0] = MFMA32(as8(KB[0][0]), Q[0][0], (f32x16){});
        Sb[0] = MFMA32(as8(KB[0][1]), Q[0][1], Sb[0]);
#define EXP4(X, o) do { X[o] = __builtin_amdgcn_exp2f(X[o]); X[o + 1] = __builtin_amdgcn_exp2f(X[o + 1]); X[o + 2] = __builtin_amdgcn_exp2f(X[o + 2]); X[o + 3] = __builtin_amdgcn_exp2f(X[o + 3]); } while (0)
#define PIN8(X) do { float a_ = X[8], b_ = X[9], c_ = X[10], d_ = X[11], e_ = X[12], f_ = X[13], g_ = X[14], h_ = X[15]; \
            asm volatile("" :: "v"(a_), "v"(b_), "v"(c_), "v"(d_), "v"(e_), "v"(f_), "v"(g_), "v"(h_)); } while (0)
#define UNIT(b, q, nb, pb, A0, A1, A2, A3) do { \
            Sb[((q) + 1) & 3] = MFMA32(as8(KB[nb][0]), Q[((q) + 1) & 3][0], (f32x16){}); SBAR(); A0; SBAR(); EXP4(Sb[q], 0); SBAR(); \
            Sb[((q) + 1) & 3] = MFMA32(as8(KB[nb][1]), Q[((q) + 1) & 3][1], Sb[((q) + 1) & 3]); SBAR(); A1; SBAR(); EXP4(Sb[q], 4); SBAR(); \
            O[((q) + 3) & 3] = smf_hi(as8(VB[pb][1]), Sb[((q) + 3) & 3], O[((q) + 3) & 3], idx); SBAR(); A2; SBAR(); EXP4(Sb[q], 8); SBAR(); \
            O[q] = smf_lo(as8(VB[b][0]), Sb[q], O[q], idx); SBAR(); A3; SBAR(); EXP4(Sb[q], 12); SBAR(); } while (0)
#define NOP_ do {} while (0)
#define XC2(v) do { ck = x3(ck, v.x, v.y); ck2 = x3(ck2, v.z, v.w); asm volatile("" : "+v"(ck), "+v"(ck2)); } while (0)
#define REDW(i) do { const unsigned ra_ = L_RED + (((w3 + 1 + (i)) & 3) * 3 + (i)) * 4096 + lane * 16; \
            _Pragma("unroll") for (int k_ = 0; k_ < 4; ++k_) *LDSP(f32x4, ra_ + k_ * 1024) = (f32x4){O[i][4 * k_], O[i][4 * k_ + 1], O[i][4 * k_ + 2], O[i][4 * k_ + 3]}; } while (0)
#define IFX(c, x) do { if (c) { x; } } while (0)
#define TILE(b, ld, val, NW, LAST) do { \
            unsigned ck = sall, ck2 = 0u; \
            UNIT(b, 0, b, ((b) + 3) & 3, IFX(ld, KVLD(KB[((b) + 3) & 3][0], kvo, 0, so)), IFX(ld, KVLD(KB[((b) + 3) & 3][1], kvo, 1024, so)), IFX(ld, KVLD(VB[((b) + 3) & 3][0], kvo, 2048, so)), IFX(ld, KVLD(VB[((b) + 3) & 3][1], kvo, 3072, so))); \
            unsigned long long bad = 0; \
            UNIT(b, 1, b, b, IFX(ld, KVLD(CK[((b) + 3) & 3], cko, 0, so)); ADV(), IFX(val, PINT(NW, ((b) + 1) & 3)), IFX(val, XC2(KB[((b) + 1) & 3][0])), IFX(val, XC2(KB[((b) + 1) & 3][1]))); \
            UNIT(b, 2, b, b, IFX(val, XC2(VB[((b) + 1) & 3][0])), IFX(val, XC2(VB[((b) + 1) & 3][1])), IFX(val, XC2(CK[((b) + 1) & 3])), IFX(val, bad = __ballot(ck != ck2); asm volatile("" : "+s"(bad)))); \
            PIN8(Sb[2]); SBAR(); \
            if (val) { if (__builtin_expect(bad != 0ull, 0)) FIXT(((b) + 1) & 3, 3); } \
            if (LAST) { REDW(0); SBAR(); } \
            UNIT(b, 3, ((b) + 1) & 3, b, NOP_, NOP_, NOP_, NOP_); } while (0)
        STAMPC(14); STAMP(13);
#pragma unroll 1
        for (int it = 0; it < 12; it += 4) { TILE(0, true, true, 10, false); TILE(1, true, true, 10, false); TILE(2, true, true, 10, false); TILE(3, true, true, 10, false); }
        FLG_SET(F_T13, 1);
        TILE(0, true, true, 10, false);
        TILE(1, false, true, 5, false); TILE(2, false, true, 0, false); TILE(3, false, false, 0, true);
        REDW(1);
        O[3] = smf_hi(as8(VB[3][1]), Sb[3], O[3], idx);
        REDW(2);
        STAMPC(15);
#undef UNIT
#undef TILE
#undef EXP4
#undef KVLD
#undef REDW
        STAMP(5);
        BAR();
        const unsigned salt = (tag * 0x9E3779B1u) | 1u;
        {
            f32x4 v[12];
#pragma unroll
            for (int s = 0; s < 3; ++s)
#pragma unroll
                for (int i = 0; i < 4; ++i) v[4 * s + i] = *LDSP(f32x4, L_RED + (w3 * 3 + s) * 4096 + i * 1024 + lane * 16);
            f32x16 o = O[3];
#pragma unroll
            for (int s = 0; s < 3; ++s)
#pragma unroll
                for (int i = 0; i < 4; ++i) { o[4 * i] += v[4 * s + i][0]; o[4 * i + 1] += v[4 * s + i][1]; o[4 * i + 2] += v[4 * s + i][2]; o[4 * i + 3] += v[4 * s + i][3]; }
            auto r = __builtin_amdgcn_permlane32_swap(__float_as_uint(o[12]), __float_as_uint(o[12]), false, false);
            const float rl = frcp(__uint_as_float(r[1]));
            unsigned d[8];
#pragma unroll
            for (int i = 0; i < 8; ++i) d[i] = pk(o[2 * i] * rl, o[2 * i + 1] * rl);
            auto sw = __builtin_amdgcn_permlane32_swap(d[6], d[7], false, false);
            v4u ra = {d[0], d[1], d[2], d[3]}, rb = {d[4], d[5], sw[0], 0u};
            *LDSP(v4u, L_OI + m * 8192 + (2 * w3) * 1024 + lane * 16) = ra;
            *LDSP(v4u, L_OI + m * 8192 + (2 * w3 + 1) * 1024 + lane * 16) = rb;
            rb.w = x8(ra, rb) ^ salt;
            BSTORE(ra, WS_O + lane * 16, (cl * 4 + m) * 8192 + w3 * 2048);
            BSTORE(rb, WS_O + 1024 + lane * 16, (cl * 4 + m) * 8192 + w3 * 2048);
        }
        STAMP(6);
        BAR();
        STAMP(7);
        BAR();
        bf16x8 WF[16];
        if (isA) {
            const v4u sg = mk_srd((w3 & 1) ? p.wu : p.wg, D * F * 2);
            const unsigned vo = lane * (F * 2) + (xcd * 28 + jj) * 16;
#pragma unroll
            for (int s = 0; s < 4; ++s) if (s < 3 || w3 < 2) dma16(sg, vo, (4 * (w3 >> 1) + s) * 64 * F * 2, lbase + L_FA + (w3 & 1) * 7168 + (4 * (w3 >> 1) + s) * 1024);
            VMWAIT(0);
            FLG_SET(F_X, 2);
            if (wave == 0) {
                FLG_WAIT(F_X, 2, 1);
                const unsigned wb = L_FA + ((lane >> 1) & 1) * 7168 + 8 * (lane & 1);
                const unsigned r0 = 16 * (gq & 1) + 4 * (gq >> 1) + q4, r1 = (gq & 1) ? 24 + 2 * (gq >> 1) + (q4 & 1) : 8 + 4 * (gq >> 1) + q4;
#pragma unroll
                for (int s = 0; s < 16; ++s) WF[s] = mk8(trd(L + wb + r0 * 16 + s * 448), trd(L + wb + r1 * 16 + s * 448));
            }
        } else if (isB) {
            FLG_WAIT(F_CO, 1, 4);
            const v4u sd = mk_srd(p.wd, D * F * 2);
            const unsigned vo = (lane >> 1) * (D * 2) + fb * 32 + (lane & 1) * 16;
#pragma unroll
            for (int s = 0; s < 14; ++s) dma16(sd, vo, (448 * w3 + 32 * s) * D * 2, lbase + L_ST + w3 * 14336 + s * 1024);
            VMWAIT(0);
            const unsigned wb = L_ST + w3 * 14336 + (8 * gq + q4) * 32 + 8 * (lane & 3);
#pragma unroll
            for (int s = 0; s < 14; ++s) WF[s] = mk8(trd(L + wb + s * 1024), trd(L + wb + s * 1024 + 128));
        }
        if (wave == 0) {
            v4u cv; unsigned spins = 0;
            FLG_WAIT(F_CO, 1, 1);
            do { POLLMEM(); cv = BLOAD(WS_CAND + lane * 128, 0); } while (!__all(cv.y == tag && cv.w == tag) && ++spins < SPIN_CAP);
            STAMP(8);
            const unsigned w0 = umax64(cv.x);
            const unsigned w1 = umax64(cv.x == w0 ? cv.z : cv.x);
            const unsigned long long b0 = __ballot(cv.x == w0), b1a = __ballot(cv.x == w1), b1b = __ballot(cv.z == w1);
            const int c0 = 2 * (int)__builtin_ctzll(b0);
            const int c1 = b1a ? 2 * (int)__builtin_ctzll(b1a) : 2 * (int)__builtin_ctzll(b1b | (1ull << 63)) + 1;
            const unsigned ro = WS_ROW + (hh ? c1 : c0) * 2048 + l31 * 32;
            if (isA) {
                v4u ra, rb; spins = 0;
                do { POLLMEM(); ra = BLOAD(ro, 0); rb = BLOAD(ro + 16, 0); } while (!__all(x8(ra, rb) == salt) && ++spins < SPIN_CAP);
                STAMP(9);
                rb.w = 0u;
                *LDSP(v4u, L_FS + lane * 32) = ra; *LDSP(v4u, L_FS + lane * 32 + 16) = rb;
                LGWAIT();
                v4u xf[16];
#pragma unroll
                for (int s = 0; s < 16; ++s) xf[s] = *LDSP(v4u, L_FS + (lane & 1) * 1024 + s * 64 + gq * 16);
                f32x4 y = {}, y2 = {};
#pragma unroll
                for (int s = 0; s < 16; s += 2) { y = MFMA16(as8(xf[s]), WF[s], y); y2 = MFMA16(as8(xf[s + 1]), WF[s + 1], y2); }
                y += y2;
                const float u0 = dppf<ROR8>(y[0]), u1 = dppf<ROR8>(y[1]);
                const float g0 = rbf(y[0]), g1 = rbf(y[1]);
                const float a0 = rbf(g0 * frcp(1.f + __expf(-g0))) * rbf(u0), a1 = rbf(g1 * frcp(1.f + __expf(-g1))) * rbf(u1);
                const float n0 = dppf<XOR1>(a0), n1 = dppf<XOR1>(a1);
                const unsigned pay = (lane & 1) ? pk(n1, a1) : pk(a0, n0);
                if (lane < 8) __hip_atomic_store((gu64*)(p.ws + WS_ACT + (xcd * 28 + jj) * 64 + (lane & 1) * 32 + (lane >> 1) * 8), ((unsigned long long)tag << 32) | pay, RLX_AGENT);
                STAMP(10);
            }
            if (lane == 0) { *CNT(C_W0) = w0; *CNT(C_W1) = w1; }
            LGWAIT();
            if (lane == 0) __hip_atomic_store(CNT(C_WINOK), 1u, __ATOMIC_RELAXED, __HIP_MEMORY_SCOPE_WORKGROUP);
            if (isB) {
                if (fb == 0 && lane == 0) __hip_atomic_store((gu32*)(p.ws + WS_EPOCH), tag, RLX_AGENT);
                v4u ra, rb, rc, rd; spins = 0;
                do { POLLMEM(); ra = BLOAD(ro, 0); rb = BLOAD(ro + 16, 0); rc = BLOAD(ro + 1024, 0); rd = BLOAD(ro + 1040, 0); }
                while (!__all(x8(ra, rb) == salt && x8(rc, rd) == salt) && ++spins < SPIN_CAP);
                *LDSP(v4u, L_FS + 8192 + lane * 32) = ra; *LDSP(v4u, L_FS + 8192 + lane * 32 + 16) = rb;
                *LDSP(v4u, L_FS + 10240 + lane * 32) = rc; *LDSP(v4u, L_FS + 10240 + lane * 32 + 16) = rd;
            }
        }
        if (isB) {
            v4u r[4]; unsigned spins = 0; bool ok;
            do {
                ok = true; POLLMEM();
#pragma unroll
                for (int i = 0; i < 4; ++i) { const int u = lane + 64 * i; r[i] = BLOAD(WS_ACT + (u < 224 ? u : 0) * 16, w3 * 3584); }
#pragma unroll
                for (int i = 0; i < 4; ++i) ok &= (r[i].y == tag && r[i].w == tag);
            } while (!__all(ok) && ++spins < SPIN_CAP);
            STAMP(11);
#pragma unroll
            for (int i = 0; i < 4; ++i) { const int u = lane + 64 * i; if (u < 224) *LDSP(v2u, L_FS + w3 * 1792 + u * 8) = (v2u){r[i].x, r[i].z}; }
            LGWAIT();
            v4u xf[14];
#pragma unroll
            for (int s = 0; s < 14; ++s) xf[s] = *LDSP(v4u, L_FS + w3 * 1792 + (4 * s + gq) * 32 + (lane & 1) * 16);
            f32x4 y = {}, y2 = {};
#pragma unroll
            for (int s = 0; s < 14; s += 2) { y = MFMA16(as8(xf[s]), WF[s], y); y2 = MFMA16(as8(xf[s + 1]), WF[s + 1], y2); }
            y += y2;
            if (lane < 16) *LDSP(f32x2, L_FS + 7168 + w3 * 128 + lane * 8) = (f32x2){y[0], y[1]};
            LGWAIT();
            unsigned arr = 0;
            if (lane == 0) arr = CNT_ADD(C_FB);
            if (__builtin_amdgcn_readfirstlane(arr) == 3u && lane < 32) {
                POLLMEM();
                float yy = 0.f;
#pragma unroll
                for (int i = 0; i < 4; ++i) yy += *LDSP(float, L_FS + 7168 + i * 128 + l15 * 8 + gq * 4);
                const unsigned wk = *CNT(C_W0 + gq);
                const int col = fb * 16 + l15, mw = col / 28, cc = col - 28 * mw;
                const int rh = cc < 24 ? (cc >> 2) & 1 : (cc - 24) >> 1, dw = cc < 24 ? (cc >> 3) * 2 + ((cc >> 1) & 1) : 6;
                const unsigned ea = L_FS + 8192 + gq * 1024 + (2 * mw + rh) * 32 + dw * 4 + (cc & 1) * 2;
                const float hn = bflo(*LDSP(uint16_t, ea)), hv = bflo(*LDSP(uint16_t, ea + 2048));
                const unsigned rb = (wk >> 16) ^ ((wk & 0x80000000u) ? 0x8000u : 0xffffu);
                const float sg = rbf(frcp(1.f + __expf(-bflo(rb))));
                const float res = hv + rbf(hn + rbf(rbf(yy) * sg));
                p.out[(size_t)(2047 - (int)(wk & 2047u)) * D + col] = __builtin_bit_cast(uint16_t, (__bf16)res);
            }
        }
        STAMP(12);
        STAMP_FLUSH();
        asm volatile(".Lfk_end_a:");
    } else {
        __builtin_amdgcn_s_setprio(2);
        const int ks0 = w3 < 3 ? 4 * w3 : 11;
        const unsigned wkv = L_WKV + (w3 < 3 ? 7168 * w3 : 20480);
        v4u xf[4];
        {
            const v4u sw = mk_srd((m & 1) ? p.wv : p.wk, D * DH * 2);
            const __amdgpu_buffer_rsrc_t xr = __builtin_amdgcn_make_buffer_rsrc((void*)(p.x + (size_t)(t0 + 16 * (m >> 1)) * D), 0, 16 * D * 2, 0x00020000);
#pragma unroll
            for (int s = 0; s < 4; ++s) xf[s] = __builtin_amdgcn_raw_buffer_load_b128(xr, l15 * (D * 2) + gq * 16 + s * 64, ks0 * 64, 0);
#pragma unroll
            for (int i = 0; i < 6; ++i) dma16(sw, lane * 16, ks0 * 1792 + i * 1024, lbase + wkv + i * 1024);
            if (w3 < 2) dma16(sw, lane * 16, ks0 * 1792 + 6144, lbase + wkv + 6144);
        }
        VMWAIT(0);
        STAMP(1);
        {
            const unsigned wa = wkv + (8 * gq + q4) * 56 + 8 * (lane & 3);
            if (w3 >= 2) xf[3] = (v4u){0u, 0u, 0u, 0u};
            v2u wf[4][4];
#pragma unroll
            for (int s = 0; s < 4; ++s) {
                const unsigned a = wa + ((s == 3 && w3 >= 2) ? 0 : s * 1792);
                wf[s][0] = trd(L + a); wf[s][1] = trd(L + a + 224); wf[s][2] = trd(L + a + 32); wf[s][3] = trd(L + a + 256);
            }
            float s1, s2;
            { float a1[2] = {0.f, 0.f}, a2[2] = {0.f, 0.f};
#pragma unroll
              for (int s = 0; s < 4; ++s)
#pragma unroll
                for (int e = 0; e < 4; ++e) { a1[e & 1] = dot2(xf[s][e], ONES2, a1[e & 1]); a2[e & 1] = dot2(xf[s][e], xf[s][e], a2[e & 1]); }
              s1 = a1[0] + a1[1]; s2 = a2[0] + a2[1]; }
            s1 = half_sum(row_sum(s1)); s2 = half_sum(row_sum(s2));
            asm volatile("" : "+v"(s1), "+v"(s2)); SBAR();
            f32x4 ka[2] = {}, ca[2] = {};
            const unsigned o3 = w3 >= 2 ? 0u : ONES2;
#pragma unroll
            for (int s = 0; s < 4; ++s) {
                const unsigned ov = s == 3 ? o3 : ONES2;
                const v4u ones = {ov, ov, ov, ov};
                const bf16x8 w0 = mk8(wf[s][0], wf[s][1]), w1 = mk8(wf[s][2], wf[s][3]);
                if (m & 1) { ka[0] = MFMA16(as8(xf[s]), w0, ka[0]); ka[1] = MFMA16(as8(xf[s]), w1, ka[1]); ca[0] = MFMA16(as8(ones), w0, ca[0]); ca[1] = MFMA16(as8(ones), w1, ca[1]); }
                else { ka[0] = MFMA16(w0, as8(xf[s]), ka[0]); ka[1] = MFMA16(w1, as8(xf[s]), ka[1]); ca[0] = MFMA16(w0, as8(ones), ca[0]); ca[1] = MFMA16(w1, as8(ones), ca[1]); }
            }
            const unsigned pb = L_KVP + w3 * 2304;
            *LDSP(f32x4, pb + lane * 16) = ka[0]; *LDSP(f32x4, pb + 1024 + lane * 16) = ka[1];
            if (m & 1) { if (lane < 16) { *LDSP(float, pb + 2048 + lane * 4) = ca[0][0]; *LDSP(float, pb + 2048 + 64 + lane * 4) = ca[1][0]; } }
            else if (l15 == 0) { *LDSP(f32x4, pb + 2048 + gq * 16) = ca[0]; *LDSP(f32x4, pb + 2048 + 64 + gq * 16) = ca[1]; }
            if (lane < 16) *LDSP(f32x2, pb + 2176 + lane * 8) = (f32x2){s1, s2};
            LGWAIT();
        }
        unsigned arr = 0;
        if (lane == 0) arr = CNT_ADD(C_KV);
        STAMP(2);
        if (__builtin_amdgcn_readfirstlane(arr) == 3u) {
            POLLMEM();
            f32x4 k0 = {}, k1 = {};
#pragma unroll
            for (int i = 0; i < 4; ++i) { k0 += *LDSP(f32x4, L_KVP + i * 2304 + lane * 16); k1 += *LDSP(f32x4, L_KVP + i * 2304 + 1024 + lane * 16); }
            v4u piece; unsigned dst;
            if (!(m & 1)) {
                f32x4 c0 = {}, c1 = {}; f32x2 ss = {};
#pragma unroll
                for (int i = 0; i < 4; ++i) { c0 += *LDSP(f32x4, L_KVP + i * 2304 + 2048 + gq * 16); c1 += *LDSP(f32x4, L_KVP + i * 2304 + 2112 + gq * 16); ss += *LDSP(f32x2, L_KVP + i * 2304 + 2176 + l15 * 8); }
                const float mean = ss.x * (1.f / D), var = fmaxf(ss.y * (1.f / D) - mean * mean, 0.f);
                const float mb = rbf(mean), rs = frcp(rbf(fsq(rbf(rbf(var) + EPS))));
                float k[8];
#pragma unroll
                for (int r = 0; r < 4; ++r) { k[r] = (k0[r] - mb * c0[r]) * rs; k[4 + r] = (k1[r] - mb * c1[r]) * rs; }
                piece = (v4u){pk(k[0], k[1]), pk(k[2], k[3]), pk(k[4], k[5]), pk(k[6], k[7])};
                if (gq == 3) { piece.z = ONES2; piece.w = 0x00003f80u; }
                dst = (gq >> 1) * 1024 + (16 * (m >> 1) + l15 + 32 * (gq & 1)) * 16;
            } else {
                float c0 = 0.f, c1 = 0.f; f32x4 sa = {}, sb = {};
#pragma unroll
                for (int i = 0; i < 4; ++i) { c0 += *LDSP(float, L_KVP + i * 2304 + 2048 + l15 * 4); c1 += *LDSP(float, L_KVP + i * 2304 + 2112 + l15 * 4);
                    sa += *LDSP(f32x4, L_KVP + i * 2304 + 2176 + gq * 32); sb += *LDSP(f32x4, L_KVP + i * 2304 + 2176 + gq * 32 + 16); }
                const float t1[4] = {sa[0], sa[2], sb[0], sb[2]}, t2[4] = {sa[1], sa[3], sb[1], sb[3]};
                float v0[4], v1[4];
#pragma unroll
                for (int r = 0; r < 4; ++r) {
                    const float mean = t1[r] * (1.f / D), var = fmaxf(t2[r] * (1.f / D) - mean * mean, 0.f);
                    const float mb = rbf(mean), rs = frcp(rbf(fsq(rbf(rbf(var) + EPS))));
                    v0[r] = (k0[r] - mb * c0) * rs; v1[r] = (k1[r] - mb * c1) * rs;
                }
                unsigned ax = pk(v0[0], v0[1]), ay = pk(v0[2], v0[3]), bx = pk(v1[0], v1[1]), by = pk(v1[2], v1[3]);
                if (l15 >= 12) { bx = by = (l15 == 12) ? ONES2 : 0u; }
                auto rx = __builtin_amdgcn_permlane16_swap(ax, bx, false, false);
                auto ry = __builtin_amdgcn_permlane16_swap(ay, by, false, false);
                piece = (v4u){rx[0], ry[0], rx[1], ry[1]};
                dst = 2048 + (m >> 1) * 1024 + lane * 16;
            }
            STAMP(10);
            BSTORE(piece, WS_KV + dst, cl * 4096);
            { const unsigned fr = dst >> 10;
              const unsigned sl = fr == 0 ? sk0 : fr == 1 ? sk1 : fr == 2 ? sv0 : sv1;
              __builtin_amdgcn_raw_buffer_store_b32(x4(piece) ^ sl, wsr, WS_CK + (dst & 1023) + fr * 4, cl * 4096, 16); }
            STAMP(13);
        }
        v2u wrq[4];
        float wsum;
        {
            float s = 0.f;
            v4u v = {0u, 0u, 0u, 0u};
            if (lane < 56) v = *(const GAS v4u*)(p.wr + lane * 8);
#pragma unroll
            for (int i = 0; i < 4; ++i) wrq[i] = *(const GAS v2u*)(p.wr + (28 * m + 7 * w3 + min(2 * i + hh, 6)) * 4);
#pragma unroll
            for (int e = 0; e < 4; ++e) s = dot2(v[e], ONES2, s);
            s += dppf<XOR1>(s); s += dppf<XOR2>(s); s += dppf<XOR7>(s); s += dppf<MIR15>(s);
            wsum = half_sum(row_sum(s));
            asm volatile("" : "+v"(wsum), "+v"(wrq[0]), "+v"(wrq[1]), "+v"(wrq[2]), "+v"(wrq[3]));
        }
        stouch<0>((const char*)p.wo + (jj * 14 + ks0) * 896, w3 < 2 ? 4 : 3);
        STAMP(3);
        __builtin_amdgcn_s_setprio(0); FLG_WAIT(F_DM, 1, 4); __builtin_amdgcn_s_setprio(3);
        __builtin_amdgcn_s_sleep(16);
        { unsigned spins = 0; while (CNT_LD(C_KV) < 4u && ++spins < SPIN_CAP) __builtin_amdgcn_s_sleep(4); POLLMEM(); }
        f32x16 acc;
        bf16x8 WO[32];
        unsigned t2 = 0, t3 = 0;
        {
            const v4u so = mk_srd(p.wo, D * D * 2), sx = mk_srd(p.x + (size_t)t0 * D, 32 * D * 2);
            const unsigned b = 56 * w3 + 32 * (gq & 1) + 8 * (lane & 3), r3 = 2 * hh + (q4 & 1);
            const unsigned a0 = (4 * hh + q4) * 256 + ((((b >> 4) + 4 * q4) & 15) << 4) + (b & 8);
            const unsigned a3 = (24 + r3) * 256 + ((((b >> 4) + 4 * r3) & 15) << 4) + (b & 8);
#define SLOT(h) ((h) < 4 ? (h) * 7168u : (h) < 7 ? 77824u + ((h) - 4) * 7168u : (h) < 9 ? 102400u + ((h) - 7) * 7168u : L_WKV + ((h) - 9) * 7168u)
#define WODMA(hd, sl) do { _Pragma("unroll") for (int s_ = 0; s_ < 7; ++s_) { dma16(so, vw, (28 * (hd) + 4 * s_) * D * 2, lbase + (sl) + s_ * 1024); VMWAIT(1); } } while (0)
#define WORD(h0, n, sl0) do { _Pragma("unroll") for (int h_ = 0; h_ < (n); ++h_) { LAS char* b_ = L + SLOT((sl0) + h_); \
                WO[2 * ((h0) + h_)] = mk8(trd(b_ + a0), trd(b_ + a0 + 2048)); WO[2 * ((h0) + h_) + 1] = mk8(trd(b_ + a0 + 4096), trd(b_ + a3)); \
                if ((h_ & 3) == 3) asm volatile("s_waitcnt lgkmcnt(0)" : "+v"(WO[2 * ((h0) + h_) - 6]), "+v"(WO[2 * ((h0) + h_) - 5]), "+v"(WO[2 * ((h0) + h_) - 4]), "+v"(WO[2 * ((h0) + h_) - 3]), \
                    "+v"(WO[2 * ((h0) + h_) - 2]), "+v"(WO[2 * ((h0) + h_) - 1]), "+v"(WO[2 * ((h0) + h_)]), "+v"(WO[2 * ((h0) + h_) + 1]) :: "memory"); } } while (0)
            constexpr unsigned SXA = 116736u;
            dma16(sx, vw, (8 * w3) * D * 2, lbase + SXA + (2 * w3) * 1024); dma16(sx, vw, (8 * w3 + 4) * D * 2, lbase + SXA + (2 * w3 + 1) * 1024);
#pragma unroll 1
            for (int i = 0; i < 3; ++i) { const int hd = w3 + 4 * i; WODMA(hd, SLOT(hd)); }
            VMWAIT(0);
            FLG_SET(F_WO, 1); FLG_WAIT(F_WO, 1, 4);
#pragma unroll
            for (int i = 0; i < 4; ++i) { const unsigned bb = 56 * w3 + 8 * min(2 * i + hh, 6);
                const v2u xr = *LDSP(v2u, SXA + l31 * 256 + ((((bb >> 4) + 4 * (l31 & 3)) & 15) << 4) + (bb & 8));
                acc[4 * i] = bflo(xr.x); acc[4 * i + 1] = bfhi(xr.x); acc[4 * i + 2] = bflo(xr.y); acc[4 * i + 3] = bfhi(xr.y); }
            asm volatile("" : "+v"(acc));
            WORD(0, 4, 0);
            FLG_SET(F_WO, 2);
            WORD(4, 8, 4);
            FLG_WAIT(F_WO, 2, 4);
            WODMA(12 + w3, w3 * 7168u);
            VMWAIT(0);
            FLG_SET(F_WO, 3); FLG_WAIT(F_WO, 3, 4);
            WORD(12, 4, 0);
#undef WODMA
#undef WORD
#undef SLOT
            STAMP(11);
            __builtin_amdgcn_s_setprio(0); FLG_WAIT(F_T13, 1, 2); __builtin_amdgcn_s_setprio(3);
            __builtin_amdgcn_s_sleep(4);
            const char* a = (const char*)p.ws + WS_O + (cl * 4 + m) * 8192 + w3 * 2048 + l15 * 128;
            if (lane == 16) a = (const char*)p.ws + WS_STAT + (cl * 4 + m) * 512 + w3 * 128;
            if (lane == 17 || lane == 18) a = (const char*)p.ws + WS_ROW + cl * 4096 + m * 1024 + w3 * 256 + (lane & 1) * 128;
            if (lane == 19) a = (const char*)p.ws + WS_CAND + ((g * 4 + w3) % 176) * 128;
            if (lane >= 20) a = (const char*)p.wd + (jj * 56 + w3 * 14 + min(lane - 20, 13)) * (D * 2) + min(xcd, 6) * 128;
            if (lane < 34) asm volatile("global_load_dword %0, %1, off" : "=v"(t2) : "v"(a) : "memory");
            const char* a2 = (const char*)((w3 & 1) ? p.wu : p.wg) + (jj * 14 + (w3 >> 1) * 7 + (lane >> 2)) * (F * 2) + min(((xcd * 448) & ~127) + (lane & 3) * 128, F * 2 - 128);
            if (lane < 28) asm volatile("global_load_dword %0, %1, off" : "=v"(t3) : "v"(a2) : "memory");
        }
        STAMP(4);
        BAR();
        BAR();
        STAMP(5);
        const unsigned salt = (tag * 0x9E3779B1u) | 1u;
#define OPROJ(mm) do { v4u of_[8]; _Pragma("unroll") for (int s_ = 0; s_ < 8; ++s_) of_[s_] = *LDSP(v4u, L_OI + (mm) * 8192 + s_ * 1024 + lane * 16); \
            _Pragma("unroll") for (int s_ = 0; s_ < 8; ++s_) acc = MFMA32(WO[8 * (mm) + s_], as8(of_[s_]), acc); } while (0)
        if (m == 0) OPROJ(0); else if (m == 1) OPROJ(1); else if (m == 2) OPROJ(2); else OPROJ(3);
        {
            v4u ra[3], rb[3]; unsigned spins = 0; bool ok;
            do {
                ok = true; POLLMEM();
#pragma unroll
                for (int i = 0; i < 3; ++i) {
                    const unsigned so = (cl * 4 + ((m + 1 + i) & 3)) * 8192 + w3 * 2048;
                    ra[i] = BLOAD(WS_O + lane * 16, so); rb[i] = BLOAD(WS_O + 1024 + lane * 16, so);
                }
#pragma unroll
                for (int i = 0; i < 3; ++i) ok &= x8(ra[i], rb[i]) == salt;
            } while (!__all(ok) && ++spins < SPIN_CAP);
#pragma unroll
            for (int i = 0; i < 3; ++i) {
                const unsigned oa = L_OI + ((m + 1 + i) & 3) * 8192 + (2 * w3) * 1024 + lane * 16;
                rb[i].w = 0u;
                *LDSP(v4u, oa) = ra[i]; *LDSP(v4u, oa + 1024) = rb[i];
            }
        }
        asm volatile("s_waitcnt vmcnt(0)" : "+v"(t2), "+v"(t3) :: "memory");
        BAR();
        STAMP(6);
        if (m != 0) OPROJ(0);
        if (m != 1) OPROJ(1);
        if (m != 2) OPROJ(2);
        if (m != 3) OPROJ(3);
#undef OPROJ
        unsigned hd[8];
        {
            float s1 = 0.f, s2 = 0.f, s3 = 0.f;
#pragma unroll
            for (int i = 0; i < 8; ++i) hd[i] = pk(acc[2 * i], acc[2 * i + 1]);
            if (hh) { hd[6] = 0u; hd[7] = 0u; }
#pragma unroll
            for (int i = 0; i < 8; ++i) { s1 = dot2(hd[i], ONES2, s1); s2 = dot2(hd[i], hd[i], s2); s3 = dot2(hd[i], wrq[i >> 1][i & 1], s3); }
            s1 = half_sum(s1); s2 = half_sum(s2); s3 = half_sum(s3);
            if (hh == 0) *LDSP(f32x4, L_KVP + w3 * 512 + l31 * 16) = (f32x4){s1, s2, s3, 0.f};
            LGWAIT();
            unsigned ar2 = 0;
            if (lane == 0) ar2 = CNT_ADD(C_ST);
            if (__builtin_amdgcn_readfirstlane(ar2) == 3u) {
                POLLMEM();
                const f32x4 a0 = *LDSP(f32x4, L_KVP + l31 * 16), a1 = *LDSP(f32x4, L_KVP + 512 + l31 * 16), a2 = *LDSP(f32x4, L_KVP + 1024 + l31 * 16), a3 = *LDSP(f32x4, L_KVP + 1536 + l31 * 16);
                v4u v = __builtin_bit_cast(v4u, (a0 + a1) + (a2 + a3)); v.w = x3(v.x, v.y, v.z) ^ salt;
                if (hh == 0) BSTORE(v, WS_STAT + l31 * 16, (cl * 4 + m) * 512);
            }
        }
        float hv[16];
#pragma unroll
        for (int i = 0; i < 8; ++i) { hv[2 * i] = bflo(hd[i]); hv[2 * i + 1] = bfhi(hd[i]); }
        v4u ha, hb;
        { auto sh = __builtin_amdgcn_permlane32_swap(hd[6], hd[7], false, false);
          ha = (v4u){hd[0], hd[1], hd[2], hd[3]}; hb = (v4u){hd[4], hd[5], sh[0], 0u}; hb.w = x8(ha, hb) ^ salt;
          asm volatile("" : "+v"(ha), "+v"(hb)); }
        STAMP(7);
        float mean_b, rsd; unsigned key;
        {
            v4u r0, r1; unsigned spins = 0;
            do { POLLMEM(); r0 = BLOAD(WS_STAT + lane * 16, cl * 2048); r1 = BLOAD(WS_STAT + 1024 + lane * 16, cl * 2048); }
            while (!__all(x4(r0) == salt && x4(r1) == salt) && ++spins < SPIN_CAP);
            STAMP(8);
            const float s1 = half_sum(__uint_as_float(r0.x) + __uint_as_float(r1.x)), s2 = half_sum(__uint_as_float(r0.y) + __uint_as_float(r1.y)), s3 = half_sum(__uint_as_float(r0.z) + __uint_as_float(r1.z));
            const float mean = s1 * (1.f / D), var = fmaxf(s2 * (1.f / D) - mean * mean, 0.f);
            mean_b = rbf(mean); rsd = frcp(rbf(fsq(rbf(rbf(var) + EPS))));
            const float rr = (s3 - mean_b * wsum) * rsd;
            const unsigned rb = __builtin_bit_cast(uint16_t, (__bf16)rr);
            key = ((rb ^ ((rb & 0x8000u) ? 0xffffu : 0x8000u)) << 16) | (unsigned)(2047 - (t0 + l31));
        }
        const unsigned k0 = umax32(key), k1 = umax32(key == k0 ? 0u : key);
        const bool cand = key == k0 || key == k1;
        if (m == 0 && wave == 4 && lane == 0) BSTORE(((v4u){k0, tag, k1, tag}), WS_CAND, cl * 128);
        unsigned nd[8], od[8];
#pragma unroll
        for (int i = 0; i < 8; ++i) {
            const unsigned c = pk(hv[2 * i] - mean_b, hv[2 * i + 1] - mean_b);
            nd[i] = pk(bflo(c) * rsd, bfhi(c) * rsd);
        }
        {
            auto sn = __builtin_amdgcn_permlane32_swap(nd[6], nd[7], false, false);
            v4u na = {nd[0], nd[1], nd[2], nd[3]}, nb = {nd[4], nd[5], sn[0], 0u};
            nb.w = x8(na, nb) ^ salt;
            if (cand) {
                const unsigned ro = WS_ROW + (cl * 2 + (key == k1)) * 2048 + ((m * 4 + w3) * 2 + hh) * 32;
                BSTORE(na, ro, 0); BSTORE(nb, ro + 16, 0); BSTORE(ha, ro + 1024, 0); BSTORE(hb, ro + 1040, 0);
            }
        }
#pragma unroll
        for (int i = 0; i < 8; ++i) od[i] = pk(hv[2 * i] + bflo(nd[i]), hv[2 * i + 1] + bfhi(nd[i]));
        FLG_SET(F_CO, 1);
        STAMP(9);
        GAS v2u* orow = (GAS v2u*)(p.out + (size_t)(t0 + l31) * D + 112 * m + 28 * w3 + 4 * hh);
        if (!cand) {
#pragma unroll
            for (int i = 0; i < 4; ++i) if (i < 3 || hh == 0) orow[2 * i] = (v2u){od[2 * i], od[2 * i + 1]};
        }
        { unsigned spins = 0; while (CNT_LD(C_WINOK) == 0u && ++spins < SPIN_CAP * 4) __builtin_amdgcn_s_sleep(4); POLLMEM(); }
        if (cand && key != *CNT(C_W0) && key != *CNT(C_W1)) {
#pragma unroll
            for (int i = 0; i < 4; ++i) if (i < 3 || hh == 0) orow[2 * i] = (v2u){od[2 * i], od[2 * i + 1]};
        }
        STAMP(12);
        STAMP_FLUSH();
        asm volatile(".Lfk_end_b:");
    }
}
}

extern "C" void kernel_launch(void* const* d_in, const int* in_sizes, int n_in, void* d_out, int out_size, void* d_ws, size_t ws_size, hipStream_t stream) {
    Params p;
    p.x = (const uint16_t*)d_in[0]; p.wq = (const uint16_t*)d_in[1]; p.wk = (const uint16_t*)d_in[2]; p.wv = (const uint16_t*)d_in[3]; p.wo = (const uint16_t*)d_in[4];
    p.wr = (const uint16_t*)d_in[6]; p.wg = (const uint16_t*)d_in[7]; p.wu = (const uint16_t*)d_in[8]; p.wd = (const uint16_t*)d_in[9];
    p.out = (uint16_t*)d_out; p.ws = (unsigned char*)d_ws;
    fused_kernel<<<NWG, NTHR, 0, stream>>>(p);
}
```

```cpp
#include <hip/hip_runtime.h>
#include <stdint.h>

#define GAS __attribute__((address_space(1)))
#define LAS __attribute__((address_space(3)))
#define CAS __attribute__((address_space(4)))
typedef unsigned v4u __attribute__((ext_vector_type(4)));
typedef unsigned v2u __attribute__((ext_vector_type(2)));
typedef float f32x16 __attribute__((ext_vector_type(16)));
typedef float f32x8 __attribute__((ext_vector_type(8)));
typedef float f32x4 __attribute__((ext_vector_type(4)));
typedef float f32x2 __attribute__((ext_vector_type(2)));
typedef __bf16 bf16x2 __attribute__((ext_vector_type(2)));
typedef __bf16 bf16x8 __attribute__((ext_vector_type(8)));
typedef __bf16 bf16x16 __attribute__((ext_vector_type(16)));
typedef short v4i16 __attribute__((ext_vector_type(4)));
typedef GAS unsigned gu32;
typedef GAS unsigned long long gu64;
#define RLX_AGENT __ATOMIC_RELAXED, __HIP_MEMORY_SCOPE_AGENT

namespace {
constexpr int S = 2048, D = 448, DH = 28, F = 1792;
constexpr int NWG = 256, NTHR = 512;
constexpr float QSCALE = 0.18898223650461361f * 1.4426950408889634f;
constexpr float EPS = 1e-5f;
constexpr unsigned SPIN_CAP = 1u << 18;
#define ACT_DELAY 16

constexpr unsigned WS_EPOCH = 0;
constexpr unsigned WS_CAND = 4096;
constexpr unsigned WS_ACT = WS_CAND + 8192;
constexpr unsigned WS_KV = 65536;
constexpr unsigned WS_CK = WS_KV + 262144;
constexpr unsigned WS_STAT = WS_CK + 262144;
constexpr unsigned WS_ROW = WS_STAT + 131072;
constexpr unsigned WS_O = WS_ROW + 128 * 2048;
constexpr unsigned WS_END = WS_O + 256 * 8192;
constexpr unsigned WS_DBG = 8u << 20;

constexpr unsigned L_XI = 0;
constexpr unsigned L_RING = 28672;
constexpr unsigned RINGB = 24576, R_QI = 22528;
constexpr unsigned L_WKV = L_RING + 4 * RINGB;
constexpr unsigned L_KVP = L_WKV + 26624;
constexpr unsigned L_STAT = L_KVP + 4 * 2304;
constexpr unsigned L_CSQ = L_STAT + 256;
constexpr unsigned L_CNT = L_CSQ + 512;
constexpr unsigned L_FLG = L_CNT + 64;
constexpr unsigned LDS_BYTES = L_FLG + 128;
constexpr unsigned L_ST = 0, L_RED = 28672, L_OI = L_RED + 49152, L_FA = L_WKV, L_FS = 57344;
static_assert(LDS_BYTES <= 163840 && L_OI + 32768 <= L_WKV, "lds");
enum { C_X = 0, C_KV = 1, C_ST = 2, C_FB = 3, C_WINOK = 4, C_WO = 5, C_W0 = 6, C_W1 = 7, C_CO = 8, C_DM = 9, C_T13 = 10 };

struct Params {
    const uint16_t* x; const uint16_t* wq; const uint16_t* wk; const uint16_t* wv; const uint16_t* wo;
    const uint16_t* wr; const uint16_t* wg; const uint16_t* wu; const uint16_t* wd;
    uint16_t* out; unsigned char* ws;
};

#define SBAR() __builtin_amdgcn_sched_barrier(0)
#define BAR() asm volatile("s_waitcnt lgkmcnt(0)\n\ts_barrier" ::: "memory")
#define VMWAIT(n) asm volatile("s_waitcnt vmcnt(" #n ")" ::: "memory")
#define LGWAIT() asm volatile("s_waitcnt lgkmcnt(0)" ::: "memory")

__device__ __forceinline__ v4u mk_srd(const void* p, unsigned bytes) {
    const unsigned long long a = (unsigned long long)p;
    v4u r; r.x = (unsigned)a; r.y = (unsigned)(a >> 32) & 0xffffu; r.z = bytes; r.w = 0x00020000u; return r;
}
__device__ __forceinline__ void dma16(v4u srd, unsigned voff, unsigned soff, unsigned ldsaddr) {
    asm volatile("s_mov_b32 m0, %0\n\ts_nop 0\n\tbuffer_load_dwordx4 %1, %2, %3 offen lds" :: "s"(ldsaddr), "v"(voff), "s"(srd), "s"(soff) : "memory");
}
__device__ __forceinline__ void dma16x4(v4u srd, unsigned voff, unsigned soff, unsigned step, unsigned ldsaddr) {
    asm volatile("s_mov_b32 m0, %0\n\ts_nop 0\n\tbuffer_load_dwordx4 %1, %2, %3 offen lds\n\tbuffer_load_dwordx4 %1, %2, %4 offen offset:1024 lds\n\t"
                 "buffer_load_dwordx4 %1, %2, %5 offen offset:2048 lds\n\tbuffer_load_dwordx4 %1, %2, %6 offen offset:3072 lds"
                 :: "s"(ldsaddr), "v"(voff), "s"(srd), "s"(soff), "s"(soff + step - 1024u), "s"(soff + 2u * step - 2048u), "s"(soff + 3u * step - 3072u) : "memory");
}
template <int IMM> __device__ __forceinline__ v4u aload16(v4u srd, unsigned voff, unsigned soff) {
    v4u r; asm volatile("buffer_load_dwordx4 %0, %1, %2, %3 offen offset:%4" : "=v"(r) : "v"(voff), "s"(srd), "s"(soff), "i"(IMM) : "memory"); return r;
}
__device__ __forceinline__ unsigned pk(float lo, float hi) {
    f32x2 v = {lo, hi}; return __builtin_bit_cast(unsigned, __builtin_convertvector(v, bf16x2));
}
__device__ __forceinline__ float bflo(unsigned u) { return __uint_as_float(u << 16); }
__device__ __forceinline__ float bfhi(unsigned u) { return __uint_as_float(u & 0xffff0000u); }
__device__ __forceinline__ float rbf(float f) { return (float)(__bf16)f; }
__device__ __forceinline__ v2u trd(LAS char* p) { return __builtin_bit_cast(v2u, __builtin_amdgcn_ds_read_tr16_b64_v4i16((LAS v4i16*)p)); }
__device__ __forceinline__ bf16x8 mk8(v2u a, v2u b) { v4u r = {a.x, a.y, b.x, b.y}; return __builtin_bit_cast(bf16x8, r); }
__device__ __forceinline__ bf16x8 as8(v4u a) { return __builtin_bit_cast(bf16x8, a); }
__device__ __forceinline__ float dot2(unsigned a, unsigned b, float c) { return __builtin_amdgcn_fdot2_f32_bf16(__builtin_bit_cast(bf16x2, a), __builtin_bit_cast(bf16x2, b), c, false); }
template <int CTRL> __device__ __forceinline__ float dppf(float x) { return __builtin_bit_cast(float, __builtin_amdgcn_mov_dpp(__builtin_bit_cast(int, x), CTRL, 0xf, 0xf, true)); }
template <int CTRL> __device__ __forceinline__ unsigned dppu(unsigned x) { return (unsigned)__builtin_amdgcn_mov_dpp((int)x, CTRL, 0xf, 0xf, true); }
constexpr int XOR1 = 0xB1, XOR2 = 0x4E, XOR7 = 0x141, ROR8 = 0x128, MIR15 = 0x140;
__device__ __forceinline__ float half_sum(float v) { auto r = __builtin_amdgcn_permlane32_swap(__float_as_uint(v), __float_as_uint(v), false, false); return __uint_as_float(r[0]) + __uint_as_float(r[1]); }
__device__ __forceinline__ unsigned umax32(unsigned v) {
    v = max(v, dppu<XOR1>(v)); v = max(v, dppu<XOR2>(v)); v = max(v, dppu<XOR7>(v)); v = max(v, dppu<MIR15>(v));
    auto r = __builtin_amdgcn_permlane16_swap(v, v, false, false); return max(r[0], r[1]);
}
__device__ __forceinline__ unsigned umax64(unsigned v) { v = umax32(v); auto r = __builtin_amdgcn_permlane32_swap(v, v, false, false); return max(r[0], r[1]); }
#define MFMA32(a, b, c) __builtin_amdgcn_mfma_f32_32x32x16_bf16(a, b, c, 0, 0, 0)
#define MFMA16(a, b, c) __builtin_amdgcn_mfma_f32_16x16x32_bf16(a, b, c, 0, 0, 0)
__device__ __forceinline__ f32x16 smf_lo(bf16x8 v, const f32x16& p, f32x16 o, int idx) {
    f32x8 h = __builtin_shufflevector(p, p, 0, 1, 2, 3, 4, 5, 6, 7);
    return __builtin_amdgcn_smfmac_f32_32x32x32_bf16(v, __builtin_bit_cast(bf16x16, h), o, idx, 0, 0);
}
__device__ __forceinline__ f32x16 smf_hi(bf16x8 v, const f32x16& p, f32x16 o, int idx) {
    f32x8 h = __builtin_shufflevector(p, p, 8, 9, 10, 11, 12, 13, 14, 15);
    return __builtin_amdgcn_smfmac_f32_32x32x32_bf16(v, __builtin_bit_cast(bf16x16, h), o, idx, 0, 0);
}
#define LDSP(T, off) ((LAS T*)(L + (off)))
#define BLOAD(off, soff) __builtin_amdgcn_raw_buffer_load_b128(wsr, (off), (soff), 16)
#define BSTORE(v, off, soff) __builtin_amdgcn_raw_buffer_store_b128((v), wsr, (off), (soff), 16)

#define STAMP(i) do {} while (0)
#define STAMPC(i) do {} while (0)
#define STAMP_DECL do {} while (0)
#define STAMP_FLUSH() do {} while (0)
#define POLLMEM() asm volatile("" ::: "memory")

__device__ __forceinline__ unsigned x3(unsigned a, unsigned b, unsigned c) { return __builtin_amdgcn_bitop3_b32(a, b, c, 0x96); }
__device__ __forceinline__ unsigned x4(v4u a) { return x3(a.x, a.y, a.z) ^ a.w; }
__device__ __forceinline__ unsigned x8(v4u a, v4u b) { return x3(x3(a.x, a.y, a.z), x3(a.w, b.x, b.y), x3(b.z, b.w, 0u)); }
__device__ __forceinline__ float frcp(float x) { return __builtin_amdgcn_rcpf(x); }
__device__ __forceinline__ float fsq(float x) { return __builtin_amdgcn_sqrtf(x); }
__device__ __forceinline__ float row_sum(float v) { auto r = __builtin_amdgcn_permlane16_swap(__float_as_uint(v), __float_as_uint(v), false, false); return __uint_as_float(r[0]) + __uint_as_float(r[1]); }
template <int KIND> __device__ __forceinline__ void stouch(const void* base, int n) {
    unsigned long long a = (unsigned long long)base;
    for (int i = 0; i < n; ++i) {
        unsigned d;
        if (KIND == 0) {
            asm volatile("s_load_dword %0, %1, 0\n\t""s_load_dword %0, %1, 64\n\t""s_load_dword %0, %1, 128\n\t""s_load_dword %0, %1, 192\n\t""s_load_dword %0, %1, 256\n\t""s_load_dword %0, %1, 320\n\t""s_load_dword %0, %1, 384\n\t""s_load_dword %0, %1, 448\n\t""s_load_dword %0, %1, 512\n\t""s_load_dword %0, %1, 576\n\t""s_load_dword %0, %1, 640\n\t""s_load_dword %0, %1, 704\n\t""s_load_dword %0, %1, 768\n\t""s_load_dword %0, %1, 832\n\t""s_waitcnt lgkmcnt(0)" : "=&s"(d) : "s"(a) : "memory"); a += 896ull;
        } else if (KIND == 1) {
            asm volatile("s_load_dword %0, %1, 0\n\t""s_load_dword %0, %1, 64\n\t""s_load_dword %0, %1, 128\n\t""s_load_dword %0, %1, 192\n\t""s_load_dword %0, %1, 256\n\t""s_load_dword %0, %1, 320\n\t""s_load_dword %0, %1, 384\n\t""s_load_dword %0, %1, 3584\n\t""s_load_dword %0, %1, 3648\n\t""s_load_dword %0, %1, 3712\n\t""s_load_dword %0, %1, 3776\n\t""s_load_dword %0, %1, 3840\n\t""s_load_dword %0, %1, 3904\n\t""s_load_dword %0, %1, 3968\n\t""s_waitcnt lgkmcnt(0)" : "=&s"(d) : "s"(a) : "memory"); a += 7168ull;
        } else {
            asm volatile("s_load_dword %0, %1, 0\n\t""s_load_dword %0, %1, 64\n\t""s_load_dword %0, %1, 896\n\t""s_load_dword %0, %1, 960\n\t""s_load_dword %0, %1, 1792\n\t""s_load_dword %0, %1, 1856\n\t""s_load_dword %0, %1, 2688\n\t""s_load_dword %0, %1, 2752\n\t""s_load_dword %0, %1, 3584\n\t""s_load_dword %0, %1, 3648\n\t""s_load_dword %0, %1, 4480\n\t""s_load_dword %0, %1, 4544\n\t""s_load_dword %0, %1, 5376\n\t""s_load_dword %0, %1, 5440\n\t""s_waitcnt lgkmcnt(0)" : "=&s"(d) : "s"(a) : "memory"); a += 6272ull;
        }
    }
}
#define CNT(i) ((LAS unsigned*)(L + L_CNT) + (i))
#define CNT_ADD(i) __hip_atomic_fetch_add(CNT(i), 1u, __ATOMIC_RELAXED, __HIP_MEMORY_SCOPE_WORKGROUP)
#define CNT_LD(i) __hip_atomic_load(CNT(i), __ATOMIC_RELAXED, __HIP_MEMORY_SCOPE_WORKGROUP)
#define ONES2 0x3f803f80u
enum { F_X = 0, F_DM = 1, F_T13 = 2, F_CO = 3, F_WO = 4, F_ST = 5, F_KV = 6 };
#define FLG_RD(f) (*(volatile LAS v4u*)(L + L_FLG + (f) * 16))
#define FLG_MIN(v) min(min((v).x, (v).y), min((v).z, (v).w))
#define FLG_SET(f, v) do { if (lane == 0) __hip_atomic_store((LAS unsigned*)(L + L_FLG + (f) * 16 + w3 * 4), (unsigned)(v), __ATOMIC_RELAXED, __HIP_MEMORY_SCOPE_WORKGROUP); } while (0)
#define FLG_WAIT(f, v, slp) do { unsigned sp_ = 0; for (;;) { POLLMEM(); const v4u f_ = *(volatile LAS v4u*)(L + L_FLG + (f) * 16); \
        if (min(min(f_.x, f_.y), min(f_.z, f_.w)) >= (unsigned)(v) || ++sp_ >= SPIN_CAP) break; if (slp) __builtin_amdgcn_s_sleep(slp); } POLLMEM(); } while (0)

extern "C" __global__ __launch_bounds__(NTHR, 2) void fused_kernel(Params p) {
    __shared__ __attribute__((aligned(16))) char lds_[LDS_BYTES];
    LAS char* L = (LAS char*)lds_;
    const unsigned lbase = (unsigned)(uintptr_t)L;
    const int g = blockIdx.x, xcd = g & 7, jj = g >> 3, cl = xcd * 8 + (jj >> 2), m = jj & 3, t0 = cl * 32;
    const int tid = threadIdx.x, lane = tid & 63, wave = __builtin_amdgcn_readfirstlane(tid >> 6);
    const int l31 = lane & 31, hh = lane >> 5, l15 = lane & 15, gq = lane >> 4, q4 = (lane & 15) >> 2;
    const int w3 = wave & 3;
    STAMP_DECL;
    STAMP(0);
    unsigned ctouch = 0;
    if (wave == 3) {
        unsigned long long base; unsigned len, len2;
        asm volatile("s_getpc_b64 %0\n.Lfk_here:\n\ts_mov_b32 %1, .Lfk_end_a-.Lfk_here\n\ts_mov_b32 %2, .Lfk_end_b-.Lfk_here" : "=s"(base), "=s"(len), "=s"(len2));
        len = max(len, len2);
        unsigned off = (unsigned)((jj & 3) * 64 + lane) * 128u;
        off = off < len - 4u ? off : 0u;
        asm volatile("global_load_dword %0, %1, %2" : "=v"(ctouch) : "v"(off), "s"(base) : "memory");
    }
    if (tid < 48) *CNT(tid) = 0u;
    BAR();
    STAMP(16);
    unsigned epoch;
    asm volatile("s_load_dword %0, %1, 0x0" : "=s"(epoch) : "s"(p.ws) : "memory");
    const __amdgpu_buffer_rsrc_t wsr = __builtin_amdgcn_make_buffer_rsrc(p.ws, 0, WS_END, 0x00020000);
    const unsigned vchunk = (l15 - 4 * gq) & 15;
    const unsigned vw = gq * (D * 2) + 224 * m + (vchunk < 14 ? vchunk : 13) * 16;
#define TAGS() asm volatile("s_waitcnt lgkmcnt(0)" : "+s"(epoch) :: "memory"); const unsigned tag = epoch + 1u; \
    const unsigned sk0 = (tag * 0x85EBCA6Bu) | 1u, sk1 = (tag * 0xC2B2AE35u) & ~1u, sv0 = (tag * 0x27D4EB2Fu) & ~1u, sv1 = (tag * 0x165667B1u) & ~1u
    const bool isA = jj < 28;
    const int fb = xcd * 4 + (jj - 28);
    const bool isB = !isA && fb < 28;

    if (wave < 4) {
        const unsigned ring = L_RING + w3 * RINGB;
        const v4u sq = mk_srd(p.wq, D * D * 2);
        {
            const v4u sx = mk_srd(p.x + (size_t)t0 * D, 32 * D * 2);
            const int row = 8 * w3 + (lane >> 3);
            const unsigned vx = row * (D * 2) + (((lane & 7) ^ ((row >> 1) & 7)) << 4);
#pragma unroll
            for (int cb = 0; cb < 7; ++cb) dma16(sx, vx, cb * 128, lbase + L_XI + (w3 * 7 + cb) * 1024);
        }
        const unsigned vq = (lane >> 2) * (D * 2) + 224 * m + ((((w3 * 7) >> 1) + (lane & 3)) << 4);
        unsigned gsrc[7];
#pragma unroll
        for (int i = 0; i < 7; ++i) { unsigned t = i + (jj >> 2); gsrc[i] = t >= 7 ? t - 7 : t; }
#define QDMA(gr) dma16x4(sq, vq, gsrc[gr] * (64 * D * 2), 16 * D * 2, lbase + ring + ((gr) % 6) * 4096)
#define QD1(gr, i) do { SBAR(); dma16(sq, vq, gsrc[gr] * (64 * D * 2) + (i) * (16 * D * 2), lbase + ring + ((gr) % 6) * 4096 + (i) * 1024); SBAR(); } while (0)
        const unsigned trq = ring + (8 * hh + q4) * 64 + 8 * (w3 & 1) + 32 * (gq & 1) + 8 * (lane & 3);
        v2u WA[7][8];
#define QRW(gr) do { _Pragma("unroll") for (int i_ = 0; i_ < 4; ++i_) { WA[gr][2 * i_] = trd(L + trq + ((4 * (gr) + i_) % 24) * 1024); WA[gr][2 * i_ + 1] = trd(L + trq + ((4 * (gr) + i_) % 24) * 1024 + 256); } } while (0)
        QDMA(0); QDMA(1);
        VMWAIT(8);
        asm volatile("" : "+v"(ctouch));
        TAGS();
        STAMP(1);
        {
            v4u v[7];
#pragma unroll
            for (int cb = 0; cb < 7; ++cb) v[cb] = *LDSP(v4u, L_XI + (w3 * 7 + cb) * 1024 + lane * 16);
            QD1(2, 0); QD1(2, 1);
            float s1[4] = {0.f, 0.f, 0.f, 0.f}, s2[4] = {0.f, 0.f, 0.f, 0.f};
#define PINS() asm volatile("" : "+v"(s1[0]), "+v"(s1[1]), "+v"(s1[2]), "+v"(s1[3]), "+v"(s2[0]), "+v"(s2[1]), "+v"(s2[2]), "+v"(s2[3]))
#define DOT(cb) do { _Pragma("unroll") for (int e = 0; e < 4; ++e) { s1[e] = dot2(v[cb][e], ONES2, s1[e]); s2[e] = dot2(v[cb][e], v[cb][e], s2[e]); } } while (0)
            DOT(0); DOT(1); PINS(); QD1(2, 2); DOT(2); DOT(3); PINS(); QD1(2, 3); DOT(4); DOT(5); PINS(); QD1(3, 0); DOT(6);
            float a = (s1[0] + s1[1]) + (s1[2] + s1[3]), b = (s2[0] + s2[1]) + (s2[2] + s2[3]);
            asm volatile("" : "+v"(a), "+v"(b)); QD1(3, 1);
            a += dppf<XOR1>(a); b += dppf<XOR1>(b); a += dppf<XOR2>(a); b += dppf<XOR2>(b); a += dppf<XOR7>(a); b += dppf<XOR7>(b);
            const float mean = a * (1.f / D), var = fmaxf(b * (1.f / D) - mean * mean, 0.f);
            float mb = rbf(mean);
            if ((lane & 7) == 0) *LDSP(f32x2, L_STAT + (8 * w3 + (lane >> 3)) * 8) = (f32x2){mb, frcp(rbf(fsq(rbf(rbf(var) + EPS))))};
            POLLMEM(); FLG_SET(F_ST, 1);
            asm volatile("" : "+v"(mb)); QD1(3, 2);
            VMWAIT(11); QRW(0);
#define CEN(cb) do { _Pragma("unroll") for (int e = 0; e < 4; ++e) v[cb][e] = pk(bflo(v[cb][e]) - mb, bfhi(v[cb][e]) - mb); \
                *LDSP(v4u, L_XI + (w3 * 7 + (cb)) * 1024 + lane * 16) = v[cb]; } while (0)
            CEN(0); QD1(3, 3); CEN(1); QD1(4, 0); CEN(2); QD1(4, 1); CEN(3); QD1(4, 2); CEN(4); QD1(4, 3); CEN(5); QD1(5, 0); CEN(6); QD1(5, 1);
#undef CEN
#undef DOT
#undef PINS
            POLLMEM(); FLG_SET(F_X, 1);
        }
        QD1(5, 2); QD1(5, 3);
        asm volatile("s_waitcnt vmcnt(12) lgkmcnt(0)" : "+v"(WA[0][0]), "+v"(WA[0][1]), "+v"(WA[0][2]), "+v"(WA[0][3]), "+v"(WA[0][4]), "+v"(WA[0][5]), "+v"(WA[0][6]), "+v"(WA[0][7]) :: "memory");
        QRW(1); QRW(2);
        QDMA(6);
        STAMP(2);
        unsigned xa[4];
#pragma unroll
        for (int i = 0; i < 4; ++i) xa[i] = L_XI + (l31 >> 3) * 7168 + (l31 & 7) * 128 + (((2 * i + hh) ^ ((l31 >> 1) & 7)) << 4);
        v4u XF[3][4];
        f32x16 acc = {};
        float qsc;
#define QRX(gr) do { _Pragma("unroll") for (int i_ = 0; i_ < 4; ++i_) XF[(gr) % 3][i_] = *LDSP(v4u, xa[i_] + gsrc[gr] * 1024); } while (0)
#define QM1(gr, i) acc = MFMA32(mk8(WA[gr][2 * (i)], WA[gr][2 * (i) + 1]), as8(XF[(gr) % 3][i]), acc)
#define QMM(gr) do { QM1(gr, 0); QM1(gr, 1); QM1(gr, 2); QM1(gr, 3); SBAR(); } while (0)
        const int rot = (g >> 2) & 15;
        v4u KB[4][2], VB[4][2], CK[4];
        const unsigned kvo = lane * 16, cko = lane * 16 + 262144;
        const v4u skf = mk_srd(p.ws + WS_KV, 524288);
        const v4u skv = skf;
        const unsigned sall = sk0 ^ sk1 ^ sv0 ^ sv1;
        unsigned so = (unsigned)(((4 * rot + w3) & 63) << 12);
#define KVLD_(dst, vo, imm, sof, srd) asm volatile("buffer_load_dwordx4 %0, %1, %2, %3 offen offset:" #imm " sc1" : "=v"(dst) : "v"(vo), "s"(srd), "s"(sof) : "memory")
#define KVLD(dst, vo, imm, sof) KVLD_(dst, vo, imm, sof, skv)
#define LDT(b, sof) do { KVLD_(KB[b][0], kvo, 0, sof, skf); KVLD_(KB[b][1], kvo, 1024, sof, skf); KVLD_(VB[b][0], kvo, 2048, sof, skf); KVLD_(VB[b][1], kvo, 3072, sof, skf); KVLD_(CK[b], cko, 0, sof, skf); } while (0)
#define ADV() so = (so + 16384u) & 0x3ffffu
#define PINT(n, b) asm volatile("s_waitcnt vmcnt(" #n ")" : "+v"(KB[b][0]), "+v"(KB[b][1]), "+v"(VB[b][0]), "+v"(VB[b][1]), "+v"(CK[b]) :: "memory")
#define CHK(b) (x3(x3(x8(KB[b][0], KB[b][1]), x8(VB[b][0], VB[b][1]), sall), x3(CK[b].x, CK[b].y, CK[b].z), CK[b].w))
#define FIXT(b, back) do { unsigned sp_ = 0; const unsigned sb_ = (so - (back) * 16384u) & 0x3ffffu; \
            do { LDT(b, sb_); PINT(0, b); } while (__any(CHK(b) != 0u) && ++sp_ < SPIN_CAP); } while (0)
        { unsigned sp = 0; for (;;) { POLLMEM(); const v4u f = FLG_RD(F_X); POLLMEM(); QRX(0); QRX(1); qsc = *LDSP(float, L_STAT + l31 * 8 + 4) * QSCALE;
            if (FLG_MIN(f) >= 1u || ++sp >= SPIN_CAP) break; } POLLMEM(); }
        VMWAIT(12); QRW(3); QRX(2); SBAR(); QMM(0);
        VMWAIT(8); QRW(4); QRX(3); SBAR(); QMM(1);
        VMWAIT(4); QRW(5); QRX(4); SBAR(); QMM(2);
        VMWAIT(0); QRW(6); QRX(5); SBAR(); QMM(3);
        QRX(6); SBAR(); QMM(4);
        QM1(5, 0); SBAR(); LDT(0, so); ADV(); SBAR(); QM1(5, 1); QM1(5, 2); SBAR(); LDT(1, so); ADV(); SBAR(); QM1(5, 3); QM1(6, 0); SBAR(); LDT(2, so); ADV(); SBAR();
        QM1(6, 1); QM1(6, 2); QM1(6, 3);
#undef QD1
#undef QRX
#undef QRW
#undef QMM
#undef QM1
#undef QDMA
        {
            unsigned qp[8];
#pragma unroll
            for (int i = 0; i < 8; ++i) qp[i] = pk(acc[2 * i] * qsc, acc[2 * i + 1] * qsc);
            if (hh) { qp[6] = 0u; qp[7] = 0u; }
            *LDSP(v4u, ring + R_QI + lane * 16) = (v4u){qp[0], qp[1], qp[4], qp[5]};
            *LDSP(v4u, ring + R_QI + 1024 + lane * 16) = (v4u){qp[2], qp[3], qp[6], qp[7]};
        }
        STAMP(3);
        POLLMEM(); FLG_SET(F_DM, 1);
        STAMP(4);
        f32x16 O[4] = {}, Sb[4];
        const int idx = (int)0xDDDDDDDDu;
        bf16x8 Q[4][2];
        PINT(10, 0);
        if (__any(CHK(0) != 0u)) FIXT(0, 3);
        { unsigned sp = 0; for (;;) { POLLMEM(); const v4u f = FLG_RD(F_DM); POLLMEM();
#pragma unroll
            for (int i = 0; i < 4; ++i) {
                const unsigned qa = L_RING + ((w3 + 1 + i) & 3) * RINGB + R_QI + lane * 16;
                Q[i][0] = as8(*LDSP(v4u, qa)); Q[i][1] = as8(*LDSP(v4u, qa + 1024));
            }
            if (FLG_MIN(f) >= 1u || ++sp >= SPIN_CAP) break; } POLLMEM(); }
        asm volatile("" : "+v"(Q[0][0]), "+v"(Q[0][1]), "+v"(Q[1][0]), "+v"(Q[1][1]), "+v"(Q[2][0]), "+v"(Q[2][1]), "+v"(Q[3][0]), "+v"(Q[3][1]));
        VB[3][1] = (v4u){0u, 0u, 0u, 0u};
        Sb[3] = (f32x16){};
        Sb[0] = MFMA32(as8(KB[0][0]), Q[0][0], (f32x16){});
        Sb[0] = MFMA32(as8(KB[0][1]), Q[0][1], Sb[0]);
#define EXP4(X, o) do { X[o] = __builtin_amdgcn_exp2f(X[o]); X[o + 1] = __builtin_amdgcn_exp2f(X[o + 1]); X[o + 2] = __builtin_amdgcn_exp2f(X[o + 2]); X[o + 3] = __builtin_amdgcn_exp2f(X[o + 3]); } while (0)
#define PIN8(X) do { float a_ = X[8], b_ = X[9], c_ = X[10], d_ = X[11], e_ = X[12], f_ = X[13], g_ = X[14], h_ = X[15]; \
            asm volatile("" :: "v"(a_), "v"(b_), "v"(c_), "v"(d_), "v"(e_), "v"(f_), "v"(g_), "v"(h_)); } while (0)
#define UNIT(b, q, nb, pb, A0, A1, A2, A3) do { \
            Sb[((q) + 1) & 3] = MFMA32(as8(KB[nb][0]), Q[((q) + 1) & 3][0], (f32x16){}); SBAR(); A0; SBAR(); EXP4(Sb[q], 0); SBAR(); \
            Sb[((q) + 1) & 3] = MFMA32(as8(KB[nb][1]), Q[((q) + 1) & 3][1], Sb[((q) + 1) & 3]); SBAR(); A1; SBAR(); EXP4(Sb[q], 4); SBAR(); \
            O[((q) + 3) & 3] = smf_hi(as8(VB[pb][1]), Sb[((q) + 3) & 3], O[((q) + 3) & 3], idx); SBAR(); A2; SBAR(); EXP4(Sb[q], 8); SBAR(); \
            O[q] = smf_lo(as8(VB[b][0]), Sb[q], O[q], idx); SBAR(); A3; SBAR(); EXP4(Sb[q], 12); SBAR(); } while (0)
#define NOP_ do {} while (0)
#define XC2(v) do { ck = x3(ck, v.x, v.y); ck2 = x3(ck2, v.z, v.w); asm volatile("" : "+v"(ck), "+v"(ck2)); } while (0)
#define REDW(i) do { const unsigned ra_ = L_RED + (((w3 + 1 + (i)) & 3) * 3 + (i)) * 4096 + lane * 16; \
            _Pragma("unroll") for (int k_ = 0; k_ < 4; ++k_) *LDSP(f32x4, ra_ + k_ * 1024) = (f32x4){O[i][4 * k_], O[i][4 * k_ + 1], O[i][4 * k_ + 2], O[i][4 * k_ + 3]}; } while (0)
#define IFX(c, x) do { if (c) { x; } } while (0)
#define TILE(b, ld, val, NW, LAST, SIG) do { \
            unsigned ck = sall, ck2 = 0u; \
            UNIT(b, 0, b, ((b) + 3) & 3, IFX(ld, KVLD(KB[((b) + 3) & 3][0], kvo, 0, so)), IFX(ld, KVLD(KB[((b) + 3) & 3][1], kvo, 1024, so)), IFX(ld, KVLD(VB[((b) + 3) & 3][0], kvo, 2048, so)), IFX(ld, KVLD(VB[((b) + 3) & 3][1], kvo, 3072, so))); \
            unsigned long long bad = 0; \
            UNIT(b, 1, b, b, IFX(ld, KVLD(CK[((b) + 3) & 3], cko, 0, so)); ADV(); IFX(SIG, FLG_SET(F_T13, 1)), IFX(val, PINT(NW, ((b) + 1) & 3)), IFX(val, XC2(KB[((b) + 1) & 3][0])), IFX(val, XC2(KB[((b) + 1) & 3][1]))); \
            UNIT(b, 2, b, b, IFX(val, XC2(VB[((b) + 1) & 3][0])), IFX(val, XC2(VB[((b) + 1) & 3][1])), IFX(val, XC2(CK[((b) + 1) & 3])), IFX(val, bad = __ballot(ck != ck2); asm volatile("" : "+s"(bad)))); \
            PIN8(Sb[2]); SBAR(); \
            if (val) { if (__builtin_expect(bad != 0ull, 0)) FIXT(((b) + 1) & 3, 3); } \
            if (LAST) { REDW(0); SBAR(); } \
            UNIT(b, 3, ((b) + 1) & 3, b, NOP_, NOP_, NOP_, NOP_); } while (0)
        STAMPC(14); STAMP(13);
#pragma unroll 1
        for (int it = 0; it < 12; it += 4) { TILE(0, true, true, 10, false, false); TILE(1, true, true, 10, false, false); TILE(2, true, true, 10, false, false); TILE(3, true, true, 10, false, false); }
        TILE(0, true, true, 10, false, true);
        TILE(1, false, true, 5, false, false); TILE(2, false, true, 0, false, false); TILE(3, false, false, 0, true, false);
        REDW(1);
        O[3] = smf_hi(as8(VB[3][1]), Sb[3], O[3], idx);
        REDW(2);
        STAMPC(15);
#undef UNIT
#undef TILE
#undef EXP4
#undef REDW
        STAMP(5);
        BAR();
        const unsigned salt = (tag * 0x9E3779B1u) | 1u;
        {
            f32x4 v[12];
#pragma unroll
            for (int s = 0; s < 3; ++s)
#pragma unroll
                for (int i = 0; i < 4; ++i) v[4 * s + i] = *LDSP(f32x4, L_RED + (w3 * 3 + s) * 4096 + i * 1024 + lane * 16);
            f32x16 o = O[3];
#pragma unroll
            for (int s = 0; s < 3; ++s)
#pragma unroll
                for (int i = 0; i < 4; ++i) { o[4 * i] += v[4 * s + i][0]; o[4 * i + 1] += v[4 * s + i][1]; o[4 * i + 2] += v[4 * s + i][2]; o[4 * i + 3] += v[4 * s + i][3]; }
            auto r = __builtin_amdgcn_permlane32_swap(__float_as_uint(o[12]), __float_as_uint(o[12]), false, false);
            float lsum = __uint_as_float(r[1]);
            f32x2 z = {0.f, 0.f};
#pragma unroll
            for (int i = 0; i < 8; ++i) z += (f32x2){o[2 * i], o[2 * i + 1]} * 0.f;
            if (__builtin_expect(__any(!(lsum >= 0x1p-64f && lsum <= 0x1p64f && z[0] + z[1] == 0.f)), 0)) {
                float mx = -__builtin_inff();
#pragma unroll 1
                for (int pass = 0; pass < 2; ++pass) {
                    o = (f32x16){};
#pragma unroll 1
                    for (unsigned t = 0; t < 64; ++t) {
                        { unsigned sp = 0; do { LDT(0, t << 12); PINT(0, 0); } while (__any(CHK(0) != 0u) && ++sp < SPIN_CAP); }
                        f32x16 sc = MFMA32(as8(KB[0][0]), Q[3][0], (f32x16){});
                        sc = MFMA32(as8(KB[0][1]), Q[3][1], sc);
                        if (pass == 0) {
#pragma unroll
                            for (int i = 0; i < 16; ++i) mx = fmaxf(mx, sc[i]);
                        } else {
#pragma unroll
                            for (int i = 0; i < 16; ++i) sc[i] = __builtin_amdgcn_exp2f(sc[i] - mx);
                            o = smf_lo(as8(VB[0][0]), sc, o, idx); o = smf_hi(as8(VB[0][1]), sc, o, idx);
                        }
                    }
                    auto rm = __builtin_amdgcn_permlane32_swap(__float_as_uint(mx), __float_as_uint(mx), false, false);
                    mx = fmaxf(__uint_as_float(rm[0]), __uint_as_float(rm[1]));
                }
                auto r2 = __builtin_amdgcn_permlane32_swap(__float_as_uint(o[12]), __float_as_uint(o[12]), false, false);
                lsum = __uint_as_float(r2[1]);
            }
            const float rl = frcp(lsum);
            unsigned d[8];
#pragma unroll
            for (int i = 0; i < 8; ++i) d[i] = pk(o[2 * i] * rl, o[2 * i + 1] * rl);
            auto sw = __builtin_amdgcn_permlane32_swap(d[6], d[7], false, false);
            v4u ra = {d[0], d[1], d[2], d[3]}, rb = {d[4], d[5], sw[0], 0u};
            *LDSP(v4u, L_OI + m * 8192 + (2 * w3) * 1024 + lane * 16) = ra;
            *LDSP(v4u, L_OI + m * 8192 + (2 * w3 + 1) * 1024 + lane * 16) = rb;
            rb.w = x8(ra, rb) ^ salt;
            BSTORE(ra, WS_O + lane * 16, (cl * 4 + m) * 8192 + w3 * 2048);
            BSTORE(rb, WS_O + 1024 + lane * 16, (cl * 4 + m) * 8192 + w3 * 2048);
        }
        STAMP(6);
        BAR();
        STAMP(7);
        BAR();
        bf16x8 WF[16];
        if (isA) {
            const v4u sg = mk_srd((w3 & 1) ? p.wu : p.wg, D * F * 2);
            const unsigned vo = lane * (F * 2) + (xcd * 28 + jj) * 16;
#pragma unroll
            for (int s = 0; s < 4; ++s) if (s < 3 || w3 < 2) dma16(sg, vo, (4 * (w3 >> 1) + s) * 64 * F * 2, lbase + L_FA + (w3 & 1) * 7168 + (4 * (w3 >> 1) + s) * 1024);
            VMWAIT(0);
            FLG_SET(F_X, 2);
            if (wave == 0) {
                FLG_WAIT(F_X, 2, 1);
                const unsigned wb = L_FA + ((lane >> 1) & 1) * 7168 + 8 * (lane & 1);
                const unsigned r0 = 16 * (gq & 1) + 4 * (gq >> 1) + q4, r1 = (gq & 1) ? 24 + 2 * (gq >> 1) + (q4 & 1) : 8 + 4 * (gq >> 1) + q4;
#pragma unroll
                for (int s = 0; s < 16; ++s) WF[s] = mk8(trd(L + wb + r0 * 16 + s * 448), trd(L + wb + r1 * 16 + s * 448));
            }
        } else if (isB) {
            const v4u sd = mk_srd(p.wd, D * F * 2);
            const unsigned vo = (lane >> 1) * (D * 2) + fb * 32 + (lane & 1) * 16;
#pragma unroll
            for (int s = 0; s < 7; ++s) dma16(sd, vo, (448 * w3 + 32 * s) * D * 2, lbase + L_ST + w3 * 14336 + s * 1024);
            FLG_WAIT(F_CO, 1, 2);
#pragma unroll
            for (int s = 7; s < 14; ++s) dma16(sd, vo, (448 * w3 + 32 * s) * D * 2, lbase + L_ST + w3 * 14336 + s * 1024);
            VMWAIT(0);
            const unsigned wb = L_ST + w3 * 14336 + (8 * gq + q4) * 32 + 8 * (lane & 3);
#pragma unroll
            for (int s = 0; s < 14; ++s) WF[s] = mk8(trd(L + wb + s * 1024), trd(L + wb + s * 1024 + 128));
        }
        float fhn = 0.f, fh = 0.f, fsg = 0.f; GAS uint16_t* fout = (GAS uint16_t*)p.out;
        if (wave == 0) {
            v4u cv; unsigned spins = 0;
            FLG_WAIT(F_CO, 1, 1);
            do { POLLMEM(); cv = BLOAD(WS_CAND + lane * 128, 0); } while (!__all(cv.y == tag && cv.w == tag) && ++spins < SPIN_CAP);
            STAMP(8);
            const unsigned w0 = umax64(cv.x);
            const unsigned w1 = umax64(cv.x == w0 ? cv.z : cv.x);
            const unsigned long long b0 = __ballot(cv.x == w0), b1a = __ballot(cv.x == w1), b1b = __ballot(cv.z == w1);
            const int c0 = 2 * (int)__builtin_ctzll(b0);
            const int c1 = b1a ? 2 * (int)__builtin_ctzll(b1a) : 2 * (int)__builtin_ctzll(b1b | (1ull << 63)) + 1;
            const unsigned ro = WS_ROW + (hh ? c1 : c0) * 2048 + l31 * 32;
            if (isA) {
                v4u ra, rb; spins = 0;
                do { POLLMEM(); ra = BLOAD(ro, 0); rb = BLOAD(ro + 16, 0); } while (!__all(x8(ra, rb) == salt) && ++spins < SPIN_CAP);
                STAMP(9);
                rb.w = 0u;
                *LDSP(v4u, L_FS + lane * 32) = ra; *LDSP(v4u, L_FS + lane * 32 + 16) = rb;
                POLLMEM();
                v4u xf[16];
#pragma unroll
                for (int s = 0; s < 16; ++s) xf[s] = *LDSP(v4u, L_FS + (lane & 1) * 1024 + s * 64 + gq * 16);
                f32x4 y = {}, y2 = {};
#pragma unroll
                for (int s = 0; s < 16; s += 2) { y = MFMA16(as8(xf[s]), WF[s], y); y2 = MFMA16(as8(xf[s + 1]), WF[s + 1], y2); }
                y += y2;
                const float u0 = dppf<ROR8>(y[0]), u1 = dppf<ROR8>(y[1]);
                const float g0 = rbf(y[0]), g1 = rbf(y[1]);
                const float a0 = rbf(g0 * frcp(1.f + __expf(-g0))) * rbf(u0), a1 = rbf(g1 * frcp(1.f + __expf(-g1))) * rbf(u1);
                const float n0 = dppf<XOR1>(a0), n1 = dppf<XOR1>(a1);
                const unsigned pay = (lane & 1) ? pk(n1, a1) : pk(a0, n0);
                if (lane < 8) __hip_atomic_store((gu64*)(p.ws + WS_ACT + (xcd * 28 + jj) * 64 + (lane & 1) * 32 + (lane >> 1) * 8), ((unsigned long long)tag << 32) | pay, RLX_AGENT);
                STAMP(10);
            }
            if (lane == 0) { *CNT(C_W0) = w0; *CNT(C_W1) = w1; POLLMEM(); __hip_atomic_store(CNT(C_WINOK), 1u, __ATOMIC_RELAXED, __HIP_MEMORY_SCOPE_WORKGROUP); }
            if (isB) {
                if (fb == 0 && lane == 0) __hip_atomic_store((gu32*)(p.ws + WS_EPOCH), tag, RLX_AGENT);
                v4u ra, rb, rc, rd; spins = 0;
                do { POLLMEM(); ra = BLOAD(ro, 0); rb = BLOAD(ro + 16, 0); rc = BLOAD(ro + 1024, 0); rd = BLOAD(ro + 1040, 0); }
                while (!__all(x8(ra, rb) == salt && x8(rc, rd) == salt) && ++spins < SPIN_CAP);
                *LDSP(v4u, L_FS + 8192 + lane * 32) = ra; *LDSP(v4u, L_FS + 8192 + lane * 32 + 16) = rb;
                *LDSP(v4u, L_FS + 10240 + lane * 32) = rc; *LDSP(v4u, L_FS + 10240 + lane * 32 + 16) = rd;
                const unsigned wk = (gq & 1) ? w1 : w0;
                const int col = fb * 16 + l15, mw = col / 28, cc = col - 28 * mw;
                const int rh = cc < 24 ? (cc >> 2) & 1 : (cc - 24) >> 1, dw = cc < 24 ? (cc >> 3) * 2 + ((cc >> 1) & 1) : 6;
                const unsigned ea = L_FS + 8192 + (gq & 1) * 1024 + (2 * mw + rh) * 32 + dw * 4 + (cc & 1) * 2;
                POLLMEM();
                fhn = bflo(*LDSP(uint16_t, ea)); fh = bflo(*LDSP(uint16_t, ea + 2048));
                const unsigned rbits = (wk >> 16) ^ ((wk & 0x80000000u) ? 0x8000u : 0xffffu);
                fsg = rbf(frcp(1.f + __expf(-bflo(rbits))));
                fout = (GAS uint16_t*)p.out + (size_t)(2047 - (int)(wk & 2047u)) * D + col;
                asm volatile("" : "+v"(fhn), "+v"(fh), "+v"(fsg), "+v"(fout));
            }
        }
        if (isB) {
            if (wave != 0) { unsigned sp = 0; while (CNT_LD(C_WINOK) == 0u && ++sp < SPIN_CAP * 4) __builtin_amdgcn_s_sleep(2); __builtin_amdgcn_s_sleep(ACT_DELAY); }
            v4u r[4]; unsigned spins = 0; bool ok;
            do {
                ok = true; POLLMEM();
#pragma unroll
                for (int i = 0; i < 4; ++i) { const int u = lane + 64 * i; r[i] = BLOAD(WS_ACT + (u < 224 ? u : 0) * 16, w3 * 3584); }
#pragma unroll
                for (int i = 0; i < 4; ++i) ok &= (r[i].y == tag && r[i].w == tag);
            } while (!__all(ok) && ++spins < SPIN_CAP);
            STAMP(11);
#pragma unroll
            for (int i = 0; i < 4; ++i) *LDSP(v2u, L_FS + w3 * 2048 + (lane + 64 * i) * 8) = (v2u){r[i].x, r[i].z};
            POLLMEM();
            v4u xf[14];
#pragma unroll
            for (int s = 0; s < 14; ++s) xf[s] = *LDSP(v4u, L_FS + w3 * 2048 + (4 * s + gq) * 32 + (lane & 1) * 16);
            f32x4 y = {}, y2 = {};
#pragma unroll
            for (int s = 0; s < 14; s += 2) { y = MFMA16(as8(xf[s]), WF[s], y); y2 = MFMA16(as8(xf[s + 1]), WF[s + 1], y2); }
            y += y2;
            if (lane < 16) *LDSP(f32x2, L_FS + 12288 + w3 * 128 + lane * 8) = (f32x2){y[0], y[1]};
            POLLMEM(); FLG_SET(F_X, 3);
            if (wave == 0) {
                float yp[4];
                { unsigned sp = 0; for (;;) { POLLMEM(); const v4u f = FLG_RD(F_X); POLLMEM();
#pragma unroll
                    for (int i = 0; i < 4; ++i) yp[i] = *LDSP(float, L_FS + 12288 + i * 128 + l15 * 8 + (gq & 1) * 4);
                    if (FLG_MIN(f) >= 3u || ++sp >= SPIN_CAP) break; } POLLMEM(); }
                const float res = fh + rbf(fhn + rbf(rbf((yp[0] + yp[1]) + (yp[2] + yp[3])) * fsg));
                if (lane < 32) *fout = __builtin_bit_cast(uint16_t, (__bf16)res);
            }
        }
        STAMP(12);
        STAMP_FLUSH();
        asm volatile(".Lfk_end_a:");
    } else {
        __builtin_amdgcn_s_setprio(2);
        const int ks0 = w3 < 3 ? 4 * w3 : 11;
        const unsigned wkv = L_WKV + (w3 < 3 ? 7168 * w3 : 20480);
        v4u xf[4];
        {
            const v4u sw = mk_srd((m & 1) ? p.wv : p.wk, D * DH * 2);
            const __amdgpu_buffer_rsrc_t xr = __builtin_amdgcn_make_buffer_rsrc((void*)(p.x + (size_t)(t0 + 16 * (m >> 1)) * D), 0, 16 * D * 2, 0x00020000);
#pragma unroll
            for (int s = 0; s < 4; ++s) xf[s] = __builtin_amdgcn_raw_buffer_load_b128(xr, l15 * (D * 2) + gq * 16 + s * 64, ks0 * 64, 0);
#pragma unroll
            for (int i = 0; i < 6; ++i) dma16(sw, lane * 16, ks0 * 1792 + i * 1024, lbase + wkv + i * 1024);
            if (w3 < 2) dma16(sw, lane * 16, ks0 * 1792 + 6144, lbase + wkv + 6144);
        }
        VMWAIT(0);
        TAGS();
        STAMP(1);
        {
            const unsigned wa = wkv + (8 * gq + q4) * 56 + 8 * (lane & 3);
            if (w3 >= 2) xf[3] = (v4u){0u, 0u, 0u, 0u};
            v2u wf[4][4];
#pragma unroll
            for (int s = 0; s < 4; ++s) {
                const unsigned a = wa + ((s == 3 && w3 >= 2) ? 0 : s * 1792);
                wf[s][0] = trd(L + a); wf[s][1] = trd(L + a + 224); wf[s][2] = trd(L + a + 32); wf[s][3] = trd(L + a + 256);
            }
            f32x4 ka[2] = {}, ca[2] = {};
            const unsigned o3 = w3 >= 2 ? 0u : ONES2;
            v4u on = {ONES2, ONES2, ONES2, ONES2}, on3 = {o3, o3, o3, o3};
            asm volatile("" : "+v"(on), "+v"(on3));
            if (m & 1) {
#pragma unroll
                for (int s = 0; s < 4; ++s) { const bf16x8 w0 = mk8(wf[s][0], wf[s][1]), w1 = mk8(wf[s][2], wf[s][3]), o = as8(s == 3 ? on3 : on);
                    ka[0] = MFMA16(as8(xf[s]), w0, ka[0]); ka[1] = MFMA16(as8(xf[s]), w1, ka[1]); ca[0] = MFMA16(o, w0, ca[0]); ca[1] = MFMA16(o, w1, ca[1]); }
            } else {
#pragma unroll
                for (int s = 0; s < 4; ++s) { const bf16x8 w0 = mk8(wf[s][0], wf[s][1]), w1 = mk8(wf[s][2], wf[s][3]), o = as8(s == 3 ? on3 : on);
                    ka[0] = MFMA16(w0, as8(xf[s]), ka[0]); ka[1] = MFMA16(w1, as8(xf[s]), ka[1]); ca[0] = MFMA16(w0, o, ca[0]); ca[1] = MFMA16(w1, o, ca[1]); }
            }
            const unsigned pb = L_KVP + w3 * 2304;
            *LDSP(f32x4, pb + lane * 16) = ka[0]; *LDSP(f32x4, pb + 1024 + lane * 16) = ka[1];
            if (m & 1) { if (lane < 16) { *LDSP(float, pb + 2048 + lane * 4) = ca[0][0]; *LDSP(float, pb + 2048 + 64 + lane * 4) = ca[1][0]; } }
            else if (l15 == 0) { *LDSP(f32x4, pb + 2048 + gq * 16) = ca[0]; *LDSP(f32x4, pb + 2048 + 64 + gq * 16) = ca[1]; }
            POLLMEM(); FLG_SET(F_KV, 1);
        }
        STAMP(2);
        if (w3 == 3) {
            f32x4 kp[8], cp[8], st0, st1;
            const unsigned ca_ = (m & 1) ? (l15 & 12) * 4 : gq * 16, sa_ = L_STAT + (16 * (m >> 1) + ((m & 1) ? 4 * gq : (l15 & 12))) * 8;
            { unsigned sp = 0; for (;;) { POLLMEM(); const v4u f = FLG_RD(F_KV), f2 = FLG_RD(F_ST); POLLMEM();
#pragma unroll
                for (int i = 0; i < 4; ++i) { kp[2 * i] = *LDSP(f32x4, L_KVP + i * 2304 + lane * 16); kp[2 * i + 1] = *LDSP(f32x4, L_KVP + i * 2304 + 1024 + lane * 16);
                    cp[2 * i] = *LDSP(f32x4, L_KVP + i * 2304 + 2048 + ca_); cp[2 * i + 1] = *LDSP(f32x4, L_KVP + i * 2304 + 2112 + ca_); }
                st0 = *LDSP(f32x4, sa_); st1 = *LDSP(f32x4, sa_ + 16);
                if (min(FLG_MIN(f), FLG_MIN(f2)) >= 1u || ++sp >= SPIN_CAP) break; } POLLMEM(); }
            const f32x4 k0 = (kp[0] + kp[2]) + (kp[4] + kp[6]), k1 = (kp[1] + kp[3]) + (kp[5] + kp[7]);
            const f32x4 c0 = (cp[0] + cp[2]) + (cp[4] + cp[6]), c1 = (cp[1] + cp[3]) + (cp[5] + cp[7]);
            v4u piece; unsigned dst;
            if (!(m & 1)) {
                const f32x4 sq_ = (lane & 2) ? st1 : st0;
                const float mb = (lane & 1) ? sq_[2] : sq_[0], rs = (lane & 1) ? sq_[3] : sq_[1];
                const f32x4 ka_ = (k0 - mb * c0) * rs, kb_ = (k1 - mb * c1) * rs;
                piece = (v4u){pk(ka_[0], ka_[1]), pk(ka_[2], ka_[3]), pk(kb_[0], kb_[1]), pk(kb_[2], kb_[3])};
                if (gq == 3) { piece.z = 0u; piece.w = 0u; }
                dst = (gq >> 1) * 1024 + (16 * (m >> 1) + l15 + 32 * (gq & 1)) * 16;
            } else {
                const float ca0 = (lane & 2) ? ((lane & 1) ? c0[3] : c0[2]) : ((lane & 1) ? c0[1] : c0[0]);
                const float ca1 = (lane & 2) ? ((lane & 1) ? c1[3] : c1[2]) : ((lane & 1) ? c1[1] : c1[0]);
                const f32x4 mb = {st0[0], st0[2], st1[0], st1[2]}, rs = {st0[1], st0[3], st1[1], st1[3]};
                const f32x4 v0 = (k0 - mb * ca0) * rs, v1 = (k1 - mb * ca1) * rs;
                unsigned ax = pk(v0[0], v0[1]), ay = pk(v0[2], v0[3]), bx = pk(v1[0], v1[1]), by = pk(v1[2], v1[3]);
                if (l15 >= 12) { bx = by = (l15 == 12) ? ONES2 : 0u; }
                auto rx = __builtin_amdgcn_permlane16_swap(ax, bx, false, false);
                auto ry = __builtin_amdgcn_permlane16_swap(ay, by, false, false);
                piece = (v4u){rx[0], ry[0], rx[1], ry[1]};
                dst = 2048 + (m >> 1) * 1024 + lane * 16;
            }
            STAMP(10);
            BSTORE(piece, WS_KV + dst, cl * 4096);
            { const unsigned fr = dst >> 10;
              const unsigned sl = fr == 0 ? sk0 : fr == 1 ? sk1 : fr == 2 ? sv0 : sv1;
              __builtin_amdgcn_raw_buffer_store_b32(x4(piece) ^ sl, wsr, WS_CK + (dst & 1023) + fr * 4, cl * 4096, 16); }
            STAMP(13);
        }
        stouch<0>(p.wr, 1);
        { unsigned d; asm volatile("s_load_dword %0, %1, 0x0\n\ts_load_dword %0, %2, 0x0\n\ts_load_dword %0, %3, 0x0\n\ts_load_dword %0, %4, 0x0\n\ts_load_dword %0, %5, 0x0\n\ts_waitcnt lgkmcnt(0)"
                                   : "=&s"(d) : "s"(p.wg), "s"(p.wu), "s"(p.wd), "s"(p.out), "s"(p.ws + WS_END - 64) : "memory"); }
        stouch<0>((const char*)p.wo + (jj * 14 + ks0) * 896, w3 < 2 ? 4 : 3);
        STAMP(3);
        __builtin_amdgcn_s_setprio(0); FLG_WAIT(F_DM, 1, 4); __builtin_amdgcn_s_setprio(3);
        __builtin_amdgcn_s_sleep(16);
        FLG_WAIT(F_KV, 1, 4);
        v2u wrq[4];
        float wsum;
        {
            float s = 0.f;
            v4u v = {0u, 0u, 0u, 0u};
            if (lane < 56) v = *(const GAS v4u*)(p.wr + lane * 8);
#pragma unroll
            for (int i = 0; i < 4; ++i) wrq[i] = *(const GAS v2u*)(p.wr + (28 * m + 7 * w3 + min(2 * i + hh, 6)) * 4);
#pragma unroll
            for (int e = 0; e < 4; ++e) s = dot2(v[e], ONES2, s);
            s += dppf<XOR1>(s); s += dppf<XOR2>(s); s += dppf<XOR7>(s); s += dppf<MIR15>(s);
            wsum = half_sum(row_sum(s));
            asm volatile("" : "+v"(wsum), "+v"(wrq[0]), "+v"(wrq[1]), "+v"(wrq[2]), "+v"(wrq[3]));
        }
        f32x16 acc;
        bf16x8 WO[32];
        unsigned t2 = 0, t3 = 0;
        {
            const v4u so = mk_srd(p.wo, D * D * 2), sx = mk_srd(p.x + (size_t)t0 * D, 32 * D * 2);
            const unsigned b = 56 * w3 + 32 * (gq & 1) + 8 * (lane & 3), r3 = 2 * hh + (q4 & 1);
            const unsigned a0 = (4 * hh + q4) * 256 + ((((b >> 4) + 4 * q4) & 15) << 4) + (b & 8);
            const unsigned a3 = (24 + r3) * 256 + ((((b >> 4) + 4 * r3) & 15) << 4) + (b & 8);
#define SLOT(h) ((h) < 4 ? (h) * 7168u : (h) < 7 ? 77824u + ((h) - 4) * 7168u : (h) < 9 ? 102400u + ((h) - 7) * 7168u : L_WKV + ((h) - 9) * 7168u)
#define WODMA(hd, sl) do { _Pragma("unroll") for (int s_ = 0; s_ < 7; ++s_) { dma16(so, vw, (28 * (hd) + 4 * s_) * D * 2, lbase + (sl) + s_ * 1024); VMWAIT(1); } } while (0)
#define WORD(h0, n, sl0) do { _Pragma("unroll") for (int h_ = 0; h_ < (n); ++h_) { LAS char* b_ = L + SLOT((sl0) + h_); \
                WO[2 * ((h0) + h_)] = mk8(trd(b_ + a0), trd(b_ + a0 + 2048)); WO[2 * ((h0) + h_) + 1] = mk8(trd(b_ + a0 + 4096), trd(b_ + a3)); \
                if ((h_ & 3) == 3) asm volatile("s_waitcnt lgkmcnt(0)" : "+v"(WO[2 * ((h0) + h_) - 6]), "+v"(WO[2 * ((h0) + h_) - 5]), "+v"(WO[2 * ((h0) + h_) - 4]), "+v"(WO[2 * ((h0) + h_) - 3]), \
                    "+v"(WO[2 * ((h0) + h_) - 2]), "+v"(WO[2 * ((h0) + h_) - 1]), "+v"(WO[2 * ((h0) + h_)]), "+v"(WO[2 * ((h0) + h_) + 1]) :: "memory"); } } while (0)
            constexpr unsigned SXA = 116736u;
            dma16(sx, vw, (8 * w3) * D * 2, lbase + SXA + (2 * w3) * 1024); dma16(sx, vw, (8 * w3 + 4) * D * 2, lbase + SXA + (2 * w3 + 1) * 1024);
#pragma unroll 1
            for (int i = 0; i < 3; ++i) { const int hd = w3 + 4 * i; WODMA(hd, SLOT(hd)); }
            VMWAIT(0);
            FLG_SET(F_WO, 1); FLG_WAIT(F_WO, 1, 4);
#pragma unroll
            for (int i = 0; i < 4; ++i) { const unsigned bb = 56 * w3 + 8 * min(2 * i + hh, 6);
                const v2u xr = *LDSP(v2u, SXA + l31 * 256 + ((((bb >> 4) + 4 * (l31 & 3)) & 15) << 4) + (bb & 8));
                acc[4 * i] = bflo(xr.x); acc[4 * i + 1] = bfhi(xr.x); acc[4 * i + 2] = bflo(xr.y); acc[4 * i + 3] = bfhi(xr.y); }
            asm volatile("" : "+v"(acc));
            WORD(0, 4, 0);
            FLG_SET(F_WO, 2);
            WORD(4, 8, 4);
            FLG_WAIT(F_WO, 2, 4);
            WODMA(12 + w3, w3 * 7168u);
            VMWAIT(0);
            FLG_SET(F_WO, 3); FLG_WAIT(F_WO, 3, 4);
            WORD(12, 4, 0);
#undef WODMA
#undef WORD
#undef SLOT
            STAMP(11);
            const char* a = (const char*)p.ws + WS_O + (cl * 4 + m) * 8192 + w3 * 2048 + l15 * 128;
            if (lane == 16) a = (const char*)p.ws + WS_STAT + (cl * 4 + m) * 512 + w3 * 128;
            if (lane == 17 || lane == 18) a = (const char*)p.ws + WS_ROW + cl * 4096 + m * 1024 + w3 * 256 + (lane & 1) * 128;
            if (lane == 19) a = (const char*)p.ws + WS_CAND + ((g * 4 + w3) % 176) * 128;
            if (lane >= 20) a = (const char*)p.wd + (jj * 56 + w3 * 14 + min(lane - 20, 13)) * (D * 2) + min(xcd, 6) * 128;
            const char* a2 = (const char*)((w3 & 1) ? p.wu : p.wg) + (jj * 14 + (w3 >> 1) * 7 + (lane >> 2)) * (F * 2) + min(((xcd * 448) & ~127) + (lane & 3) * 128, F * 2 - 128);
            asm volatile("" : "+v"(a), "+v"(a2));
            FLG_WAIT(F_T13, 1, 3);
            if (lane < 34) asm volatile("global_load_dword %0, %1, off" : "=v"(t2) : "v"(a) : "memory");
            if (lane < 28) asm volatile("global_load_dword %0, %1, off" : "=v"(t3) : "v"(a2) : "memory");
        }
        STAMP(4);
        BAR();
        BAR();
        STAMP(5);
        const unsigned salt = (tag * 0x9E3779B1u) | 1u;
#define OPROJ(mm) do { v4u of_[8]; _Pragma("unroll") for (int s_ = 0; s_ < 8; ++s_) of_[s_] = *LDSP(v4u, L_OI + (mm) * 8192 + s_ * 1024 + lane * 16); \
            _Pragma("unroll") for (int s_ = 0; s_ < 8; ++s_) acc = MFMA32(WO[8 * (mm) + s_], as8(of_[s_]), acc); } while (0)
        if (m == 0) OPROJ(0); else if (m == 1) OPROJ(1); else if (m == 2) OPROJ(2); else OPROJ(3);
        {
            v4u ra[3], rb[3]; unsigned spins = 0; bool ok;
            do {
                ok = true; POLLMEM();
#pragma unroll
                for (int i = 0; i < 3; ++i) {
                    const unsigned so = (cl * 4 + ((m + 1 + i) & 3)) * 8192 + w3 * 2048;
                    ra[i] = BLOAD(WS_O + lane * 16, so); rb[i] = BLOAD(WS_O + 1024 + lane * 16, so);
                }
#pragma unroll
                for (int i = 0; i < 3; ++i) ok &= x8(ra[i], rb[i]) == salt;
            } while (!__all(ok) && ++spins < SPIN_CAP);
#pragma unroll
            for (int i = 0; i < 3; ++i) {
                const unsigned oa = L_OI + ((m + 1 + i) & 3) * 8192 + (2 * w3) * 1024 + lane * 16;
                rb[i].w = 0u;
                *LDSP(v4u, oa) = ra[i]; *LDSP(v4u, oa + 1024) = rb[i];
            }
        }
        asm volatile("s_waitcnt vmcnt(0)" : "+v"(t2), "+v"(t3) :: "memory");
        BAR();
        STAMP(6);
        if (m != 0) OPROJ(0);
        if (m != 1) OPROJ(1);
        if (m != 2) OPROJ(2);
        if (m != 3) OPROJ(3);
#undef OPROJ
        unsigned hd[8];
        {
            float s1 = 0.f, s2 = 0.f, s3 = 0.f;
#pragma unroll
            for (int i = 0; i < 8; ++i) hd[i] = pk(acc[2 * i], acc[2 * i + 1]);
            if (hh) { hd[6] = 0u; hd[7] = 0u; }
#pragma unroll
            for (int i = 0; i < 8; ++i) { s1 = dot2(hd[i], ONES2, s1); s2 = dot2(hd[i], hd[i], s2); s3 = dot2(hd[i], wrq[i >> 1][i & 1], s3); }
            s1 = half_sum(s1); s2 = half_sum(s2); s3 = half_sum(s3);
            if (hh == 0) *LDSP(f32x4, L_KVP + w3 * 512 + l31 * 16) = (f32x4){s1, s2, s3, 0.f};
            LGWAIT();
            unsigned ar2 = 0;
            if (lane == 0) ar2 = CNT_ADD(C_ST);
            if (__builtin_amdgcn_readfirstlane(ar2) == 3u) {
                POLLMEM();
                const f32x4 a0 = *LDSP(f32x4, L_KVP + l31 * 16), a1 = *LDSP(f32x4, L_KVP + 512 + l31 * 16), a2 = *LDSP(f32x4, L_KVP + 1024 + l31 * 16), a3 = *LDSP(f32x4, L_KVP + 1536 + l31 * 16);
                v4u v = __builtin_bit_cast(v4u, (a0 + a1) + (a2 + a3)); v.w = x3(v.x, v.y, v.z) ^ salt;
                if (hh == 0) BSTORE(v, WS_STAT + l31 * 16, (cl * 4 + m) * 512);
            }
        }
        float hv[16];
#pragma unroll
        for (int i = 0; i < 8; ++i) { hv[2 * i] = bflo(hd[i]); hv[2 * i + 1] = bfhi(hd[i]); }
        v4u ha, hb;
        { auto sh = __builtin_amdgcn_permlane32_swap(hd[6], hd[7], false, false);
          ha = (v4u){hd[0], hd[1], hd[2], hd[3]}; hb = (v4u){hd[4], hd[5], sh[0], 0u}; hb.w = x8(ha, hb) ^ salt;
          asm volatile("" : "+v"(ha), "+v"(hb)); }
        STAMP(7);
        float mean_b, rsd; unsigned key;
        {
            v4u r0, r1; unsigned spins = 0;
            do { POLLMEM(); r0 = BLOAD(WS_STAT + lane * 16, cl * 2048); r1 = BLOAD(WS_STAT + 1024 + lane * 16, cl * 2048); }
            while (!__all(x4(r0) == salt && x4(r1) == salt) && ++spins < SPIN_CAP);
            STAMP(8);
            const float s1 = half_sum(__uint_as_float(r0.x) + __uint_as_float(r1.x)), s2 = half_sum(__uint_as_float(r0.y) + __uint_as_float(r1.y)), s3 = half_sum(__uint_as_float(r0.z) + __uint_as_float(r1.z));
            const float mean = s1 * (1.f / D), var = fmaxf(s2 * (1.f / D) - mean * mean, 0.f);
            mean_b = rbf(mean); rsd = frcp(rbf(fsq(rbf(rbf(var) + EPS))));
            const float rr = (s3 - mean_b * wsum) * rsd;
            const unsigned rb = __builtin_bit_cast(uint16_t, (__bf16)rr);
            key = ((rb ^ ((rb & 0x8000u) ? 0xffffu : 0x8000u)) << 16) | (unsigned)(2047 - (t0 + l31));
        }
        const unsigned k0 = umax32(key), k1 = umax32(key == k0 ? 0u : key);
        const bool cand = key == k0 || key == k1;
        if (m == 0 && wave == 4 && lane == 0) BSTORE(((v4u){k0, tag, k1, tag}), WS_CAND, cl * 128);
        unsigned nd[8], od[8];
#pragma unroll
        for (int i = 0; i < 8; ++i) {
            const unsigned c = pk(hv[2 * i] - mean_b, hv[2 * i + 1] - mean_b);
            nd[i] = pk(bflo(c) * rsd, bfhi(c) * rsd);
        }
        {
            auto sn = __builtin_amdgcn_permlane32_swap(nd[6], nd[7], false, false);
            v4u na = {nd[0], nd[1], nd[2], nd[3]}, nb = {nd[4], nd[5], sn[0], 0u};
            nb.w = x8(na, nb) ^ salt;
            if (cand) {
                const unsigned ro = WS_ROW + (cl * 2 + (key == k1)) * 2048 + ((m * 4 + w3) * 2 + hh) * 32;
                BSTORE(na, ro, 0); BSTORE(nb, ro + 16, 0); BSTORE(ha, ro + 1024, 0); BSTORE(hb, ro + 1040, 0);
            }
        }
#pragma unroll
        for (int i = 0; i < 8; ++i) od[i] = pk(hv[2 * i] + bflo(nd[i]), hv[2 * i + 1] + bfhi(nd[i]));
        FLG_SET(F_CO, 1);
        STAMP(9);
        GAS v2u* orow = (GAS v2u*)(p.out + (size_t)(t0 + l31) * D + 112 * m + 28 * w3 + 4 * hh);
        if (!cand) {
#pragma unroll
            for (int i = 0; i < 4; ++i) if (i < 3 || hh == 0) orow[2 * i] = (v2u){od[2 * i], od[2 * i + 1]};
        }
        { unsigned spins = 0; while (CNT_LD(C_WINOK) == 0u && ++spins < SPIN_CAP * 4) __builtin_amdgcn_s_sleep(4); POLLMEM(); }
        if (cand && key != *CNT(C_W0) && key != *CNT(C_W1)) {
#pragma unroll
            for (int i = 0; i < 4; ++i) if (i < 3 || hh == 0) orow[2 * i] = (v2u){od[2 * i], od[2 * i + 1]};
        }
        STAMP(12);
        STAMP_FLUSH();
        asm volatile(".Lfk_end_b:");
    }
}
}

extern "C" void kernel_launch(void* const* d_in, const int* in_sizes, int n_in, void* d_out, int out_size, void* d_ws, size_t ws_size, hipStream_t stream) {
    Params p;
    p.x = (const uint16_t*)d_in[0]; p.wq = (const uint16_t*)d_in[1]; p.wk = (const uint16_t*)d_in[2]; p.wv = (const uint16_t*)d_in[3]; p.wo = (const uint16_t*)d_in[4];
    p.wr = (const uint16_t*)d_in[6]; p.wg = (const uint16_t*)d_in[7]; p.wu = (const uint16_t*)d_in[8]; p.wd = (const uint16_t*)d_in[9];
    p.out = (uint16_t*)d_out; p.ws = (unsigned char*)d_ws;
    fused_kernel<<<NWG, NTHR, 0, stream>>>(p);
}
```
